```python
import torch

def _softmax(x, axis=-1):
    return torch.softmax(x, dim=axis)
def _gelu(x):
    return torch.nn.functional.gelu(x, approximate="tanh")
def _silu(x):
    return torch.nn.functional.silu(x)
def _sigmoid(x):
    return torch.sigmoid(x)
def _layernorm(x, axis=-1, eps=1e-5):
    m = x.mean(dim=axis, keepdim=True)
    v = x.var(dim=axis, keepdim=True, unbiased=False)
    return (x - m) / torch.sqrt(v + eps)
def _scatter_add(tgt, idx, src, axis=0):
    return tgt.clone().index_add_(axis, idx, src)
def _segment_sum(src, seg, n):
    out = torch.zeros((n,) + src.shape[1:], dtype=src.dtype)
    return out.index_add_(0, seg, src)
def _pad(x, pw):
    flat = tuple(v for pair in reversed(pw) for v in pair)
    return torch.nn.functional.pad(x, flat)

kernel_name = 'standalone__cheap_equivariant_egnn__graph'

def setup_inputs():

    _w = (lambda a: (lambda t: t.to(torch.bfloat16) if t.is_floating_point() else t)(torch.as_tensor(a)))
    N, deg, D, De = 50000, 16, 64, max(16, 1)
    E = N * deg
    return {
        "h": _w((lambda *s: torch.randn(*s, dtype=torch.bfloat16) * 0.2)(N, D)),
        "pos": _w((lambda *s: torch.randn(*s, dtype=torch.bfloat16) * 0.8)(N, 3)),
        "e_attr": _w((lambda *s: torch.randn(*s, dtype=torch.bfloat16) * 0.8)(E, De)),
        "src": _w((lambda lo, hi, *s: torch.randint(lo, hi, s))(0, N, E).to(torch.int64)),
        "dst": _w((lambda lo, hi, *s: torch.randint(lo, hi, s))(0, N, E).to(torch.int64)),
        "w_e": _w(0.1 * (lambda *s: torch.randn(*s, dtype=torch.bfloat16) * 0.8)(2 * D + 1 + De, D)),
        "w_x": _w(0.1 * (lambda *s: torch.randn(*s, dtype=torch.bfloat16) * 0.2)(D, 1)),
        "w_h": _w(0.1 * (lambda *s: torch.randn(*s, dtype=torch.bfloat16) * 0.2)(2 * D, D)),
    }

def reference(h, pos, e_attr, src, dst, w_e, w_x, w_h):
    N, D = h.shape[0], h.shape[1]
    diff = pos[dst] - pos[src]
    dist2 = torch.sum(diff * diff, axis=-1, keepdims=True)
    e_in = torch.concatenate([h[dst], h[src], dist2, e_attr], axis=-1)
    m_ij = _silu(e_in @ w_e)
    coord_scale = m_ij @ w_x
    pos_new = pos + _segment_sum(diff * coord_scale, dst, N)
    m_i = _segment_sum(m_ij, dst, N)
    h_new = h + _silu(torch.concatenate([h, m_i], axis=-1) @ w_h)
    return h_new, pos_new
```

```cpp
#include <hip/hip_runtime.h>
#include <stdint.h>
#include <type_traits>

namespace {

typedef __bf16 bf16x8 __attribute__((ext_vector_type(8)));
typedef __bf16 bf16x2 __attribute__((ext_vector_type(2)));
typedef float f32x16 __attribute__((ext_vector_type(16)));
typedef float f32x4 __attribute__((ext_vector_type(4)));
typedef unsigned u32x4 __attribute__((ext_vector_type(4)));
typedef unsigned u32x2 __attribute__((ext_vector_type(2)));
typedef unsigned u32x8 __attribute__((ext_vector_type(8)));
typedef unsigned u32x16 __attribute__((ext_vector_type(16)));
typedef unsigned u32x32 __attribute__((ext_vector_type(32)));
typedef short s16x4 __attribute__((ext_vector_type(4)));
typedef __attribute__((address_space(3))) s16x4* lds_s16x4_p;

#define DI __device__ __forceinline__
#define MFMA(a, b, c) __builtin_amdgcn_mfma_f32_32x32x16_bf16((a), (b), (c), 0, 0, 0)

#define POLL_SLEEP 2
#define HPF 0
#define EPF 2
#define OUT_AUX 16
constexpr int NBK = 256;
constexpr int LG = 16;
constexpr int NB = 196;
constexpr int CH = 3136;
constexpr float LOG2E = 1.4426950408889634f;
constexpr float LN2 = 0.6931471805599453f;

constexpr size_t WS_EPOCH = 0;
constexpr size_t WS_TAB = 256;
constexpr size_t WS_REC = WS_TAB + (size_t)NBK * NBK * 8;

constexpr int W_STH = 0, W_STE = 4096, W_STP = 5120, W_SI = 5376, W_TI = W_SI + 26 * 80, WSZ = W_TI + 320;
constexpr int DUMMY = 25;
constexpr int MIP = 144, MISZ = 32 * MIP;
constexpr int MIL = 80;
constexpr int SEG = 17 * 256;
constexpr int L_CNT = 0;
constexpr int L_BASE = L_CNT + 2048;
constexpr int L_FLAG = L_CNT + 512;
constexpr int L_REC = L_BASE + 2048;
constexpr int L_LUT = L_REC + 8 * SEG + 512;
constexpr int L_WAVE = L_LUT + 26 * 80;
constexpr int L_MI = L_WAVE + 8 * WSZ;
constexpr int L_WJ = L_MI + 8 * MISZ;
constexpr int L_WH = L_WJ + 6 * 1024;
constexpr int L_TOTAL = L_WH + 16 * 1024;
static_assert(L_TOTAL <= 163840, "LDS");
static_assert(WSZ % 16 == 0 && L_MI % 16 == 0 && L_LUT % 16 == 0, "align");

#pragma clang diagnostic ignored "-Wint-to-pointer-cast"
#define LDS(T, off) (*(__attribute__((address_space(3))) T*)(unsigned)(off))
DI unsigned mul24(unsigned a, unsigned b) { return (unsigned)__umul24(a, b); }
DI float bf2f(unsigned lo16) { return __uint_as_float(lo16 << 16); }
DI unsigned pk_bf16(float a, float b) { bf16x2 v = {(__bf16)a, (__bf16)b}; return __builtin_bit_cast(unsigned, v); }
DI bf16x8 as_bf(u32x4 v) { return __builtin_bit_cast(bf16x8, v); }
DI int opaque(int x) { asm volatile("" : "+v"(x)); return x; }
DI int lane_id() {
  int x; asm volatile("v_mbcnt_lo_u32_b32 %0, -1, 0\n\tv_mbcnt_hi_u32_b32 %0, -1, %0" : "=v"(x)); return x;
}
DI void wait_vm0() { asm volatile("s_waitcnt vmcnt(0)" ::: "memory"); }
DI void wait_lgkm0() { asm volatile("s_waitcnt lgkmcnt(0)" ::: "memory"); }
DI void barrier_lds() { asm volatile("s_waitcnt lgkmcnt(0)\n\ts_barrier" ::: "memory"); }
DI void dma16(const void* g, unsigned off, unsigned l) {
  unsigned keep;
  asm volatile("s_mov_b32 %0, m0\n\ts_mov_b32 m0, %3\n\ts_nop 0\n\tglobal_load_lds_dwordx4 %1, %2\n\ts_mov_b32 m0, %0"
               : "=&s"(keep) : "v"(off), "s"(g), "s"(l) : "memory");
}
DI void dma4(const void* g, unsigned off, unsigned l) {
  unsigned keep;
  asm volatile("s_mov_b32 %0, m0\n\ts_mov_b32 m0, %3\n\ts_nop 0\n\tglobal_load_lds_dword %1, %2\n\ts_mov_b32 m0, %0"
               : "=&s"(keep) : "v"(off), "s"(g), "s"(l) : "memory");
}
DI void dma_tile(const void* hm, const void* ea, const void* ps, unsigned o0, unsigned o1, unsigned o2, unsigned o3,
                 unsigned oe, unsigned op, unsigned l) {
  unsigned keep;
  asm volatile("s_mov_b32 %0, m0\n\t"
               "s_add_u32 m0, %10, 0x1000\n\ts_nop 0\n\tglobal_load_lds_dwordx4 %5, %8\n\t"
               "s_mov_b32 m0, %10\n\ts_nop 0\n\tglobal_load_lds_dwordx4 %1, %7\n\t"
               "s_add_u32 m0, %10, 0x400\n\ts_nop 0\n\tglobal_load_lds_dwordx4 %2, %7\n\t"
               "s_add_u32 m0, %10, 0x800\n\ts_nop 0\n\tglobal_load_lds_dwordx4 %3, %7\n\t"
               "s_add_u32 m0, %10, 0xc00\n\ts_nop 0\n\tglobal_load_lds_dwordx4 %4, %7\n\t"
               "s_add_u32 m0, %10, 0x1400\n\ts_nop 0\n\tglobal_load_lds_dword %6, %9\n\t"
               "s_mov_b32 m0, %0"
               : "=&s"(keep) : "v"(o0), "v"(o1), "v"(o2), "v"(o3), "v"(oe), "v"(op), "s"(hm), "s"(ea), "s"(ps), "s"(l)
               : "memory", "scc");
}
template <int CTRL, int ROWMASK> DI unsigned dpp_add(unsigned x) {
  return x + (unsigned)__builtin_amdgcn_update_dpp(0, (int)x, CTRL, ROWMASK, 0xf, false);
}
DI unsigned wave_scan_incl(unsigned x) {
  x = dpp_add<0x111, 0xf>(x); x = dpp_add<0x112, 0xf>(x); x = dpp_add<0x114, 0xf>(x); x = dpp_add<0x118, 0xf>(x);
  x = dpp_add<0x142, 0xa>(x); x = dpp_add<0x143, 0xc>(x);
  return x;
}
#define STAMP(i)
#define STAMP_ARG

#define CLOBBER_V "v88", "v89", "v90", "v91", "v92", "v93", "v94", "v95", "v96", "v97", "v98", "v99", "v100", "v101", "v102", "v103", "v104", "v105", "v106", "v107", "v108", "v109", "v110", "v111", "v112", "v113", "v114", "v115", "v116", "v117", "v118", "v119", "v120", "v121", "v122", "v123", "v124", "v125", "v126", "v127", "v128", "v129", "v130", "v131", "v132", "v133", "v134", "v135", "v136", "v137", "v138", "v139", "v140", "v141", "v142", "v143", "v144", "v145", "v146", "v147", "v148", "v149", "v150", "v151", "v152", "v153", "v154", "v155", "v156", "v157", "v158", "v159", "v160", "v161", "v162", "v163", "v164", "v165", "v166", "v167", "v168", "v169", "v170", "v171", "v172", "v173", "v174", "v175", "v176", "v177", "v178", "v179", "v180", "v181", "v182", "v183", "v184", "v185", "v186", "v187", "v188", "v189", "v190", "v191", "v192", "v193", "v194", "v195", "v196", "v197", "v198", "v199", "v200", "v201", "v202", "v203", "v204", "v205", "v206", "v207", "v208", "v209", "v210", "v211", "v212", "v213", "v214", "v215"
#define LOOP_ASM \
  "s_mov_b32 %[keep], m0\n\t" \
  "ds_read_u16 v216, v248 offset:0\n\t" \
  "ds_read_u16 v218, v248 offset:8\n\t" \
  "ds_read_u16 v220, v248 offset:16\n\t" \
  "ds_read_u16 v222, v248 offset:24\n\t" \
  "ds_read_b32 v224, v249\n\t" \
  "ds_read_u16 v226, v249 offset:4\n\t" \
  "ds_read_u16 v228, v250\n\t" \
  "ds_read_b128 v[96:99], v246 offset:0\n\t" \
  "ds_read_b128 v[92:95], v246 offset:1024\n\t" \
  "v_mov_b32 v192, 0\n\t" \
  "v_mov_b32 v193, 0\n\t" \
  "v_mov_b32 v194, 0\n\t" \
  "v_mov_b32 v195, 0\n\t" \
  "v_mov_b32 v196, 0\n\t" \
  "v_mov_b32 v197, 0\n\t" \
  "v_mov_b32 v198, 0\n\t" \
  "v_mov_b32 v199, 0\n\t" \
  "v_mov_b32 v214, 0\n\t" \
  "v_mov_b32 v215, 0\n\t" \
  "v_mov_b32 v88, 0\n\t" \
  "v_add_u32 v248, 0x100, v248\n\t" \
  "v_add_u32 v249, 0x100, v249\n\t" \
  "v_add_u32 v250, 0x100, v250\n\t" \
  "s_waitcnt lgkmcnt(0)\n\t" \
  "v_add_u32 v230, v247, v228\n\t" \
  "v_add_u32 v231, v239, v228\n\t" \
  "ds_read_b64 v[208:209], v230\n\t" \
  "ds_read_b128 v[188:191], v231 offset:32\n\t" \
  "ds_read_b128 v[184:187], v231\n\t" \
  "v_add_u32 v229, v240, v228\n\t" \
  "ds_write_b16_d16_hi v229, v254 offset:5376\n\t" \
  "s_waitcnt vmcnt(0)\n\t" \
  "ds_read_b128 v[164:167], v232\n\t" \
  "ds_read_b128 v[168:171], v233\n\t" \
  "ds_read_b128 v[172:175], v234\n\t" \
  "ds_read_b128 v[176:179], v235\n\t" \
  "ds_read_b128 v[180:183], v236 offset:4096\n\t" \
  "ds_read_u16_d16_hi v210, v238 offset:5120\n\t" \
  "ds_read_u16_d16_hi v211, v237 offset:5126\n\t" \
  "s_waitcnt lgkmcnt(0)\n\t" \
  "s_cmp_lt_i32 %[nt], 1\n\t" \
  "s_cbranch_scc1 7f\n\t" \
  "s_add_u32 m0, %[b1], 4096\n\t" \
  "s_nop 0\n\t" \
  "buffer_load_dwordx4 v[224:225], %[rse], 0 idxen offen lds\n\t" \
  "buffer_load_dword v[226:227], %[rsp], 0 idxen offen offset:1024 lds\n\t" \
  "s_mov_b32 m0, %[b1]\n\t" \
  "s_nop 0\n\t" \
  "buffer_load_dwordx4 v[216:217], %[rsh], 0 idxen offen  lds\n\t" \
  "buffer_load_dwordx4 v[218:219], %[rsh], 0 idxen offen offset:1024 lds\n\t" \
  "buffer_load_dwordx4 v[220:221], %[rsh], 0 idxen offen offset:2048 lds\n\t" \
  "buffer_load_dwordx4 v[222:223], %[rsh], 0 idxen offen offset:3072 lds\n\t" \
  "7:\n\t" \
  "ds_read_u16 v216, v248 offset:0\n\t" \
  "ds_read_u16 v218, v248 offset:8\n\t" \
  "ds_read_u16 v220, v248 offset:16\n\t" \
  "ds_read_u16 v222, v248 offset:24\n\t" \
  "ds_read_b32 v224, v249\n\t" \
  "ds_read_u16 v226, v249 offset:4\n\t" \
  "ds_read_u16 v228, v250\n\t" \
  "v_add_u32 v250, 0x100, v250\n\t" \
  "v_sub_f32 v212, v208, v210\n\t" \
  "v_sub_f32 v213, v209, v211\n\t" \
  "v_mul_f32 v208, v212, v212\n\t" \
  "v_mul_f32 v209, v213, v213\n\t" \
  "v_cvt_pk_bf16_f32 v191, v209, v208\n\t" \
  "v_mfma_f32_32x32x16_bf16 v[100:115], v[36:39], v[184:187], 0\n\t" \
  "v_mfma_f32_32x32x16_bf16 v[100:115], v[4:7], v[164:167], v[100:115]\n\t" \
  "v_mfma_f32_32x32x16_bf16 v[100:115], v[12:15], v[168:171], v[100:115]\n\t" \
  "v_mfma_f32_32x32x16_bf16 v[100:115], v[20:23], v[172:175], v[100:115]\n\t" \
  "v_mfma_f32_32x32x16_bf16 v[100:115], v[28:31], v[176:179], v[100:115]\n\t" \
  "v_mfma_f32_32x32x16_bf16 v[100:115], v[96:99], v[180:183], v[100:115]\n\t" \
  "v_mfma_f32_32x32x16_bf16 v[100:115], v[44:47], v[188:191], v[100:115]\n\t" \
  "s_cmp_eq_u32 %[nt], 0\n\t" \
  "s_cbranch_scc1 6f\n\t" \
  "s_nop 10\n\t" \
  "v_exp_f32 v132, v100\n\t" \
  "v_exp_f32 v133, v101\n\t" \
  "v_exp_f32 v134, v102\n\t" \
  "v_exp_f32 v135, v103\n\t" \
  "v_exp_f32 v136, v104\n\t" \
  "v_exp_f32 v137, v105\n\t" \
  "v_exp_f32 v138, v106\n\t" \
  "v_exp_f32 v139, v107\n\t" \
  "v_exp_f32 v140, v108\n\t" \
  "v_exp_f32 v141, v109\n\t" \
  "v_exp_f32 v142, v110\n\t" \
  "v_exp_f32 v143, v111\n\t" \
  "v_exp_f32 v144, v112\n\t" \
  "v_exp_f32 v145, v113\n\t" \
  "v_exp_f32 v146, v114\n\t" \
  "v_exp_f32 v147, v115\n\t" \
  "s_nop 1\n\t" \
  "v_mfma_f32_32x32x8bf16_1k v[132:147], v[252:253], v[252:253], v[132:147]\n\t" \
  "v_mfma_f32_32x32x16_bf16 v[116:131], v[40:43], v[184:187], 0\n\t" \
  "v_mfma_f32_32x32x16_bf16 v[116:131], v[48:51], v[188:191], v[116:131]\n\t" \
  "s_cmp_lt_i32 %[thr], 0\n\t" \
  "s_cbranch_scc1 1f\n\t" \
  "s_setprio 1\n\t" \
  "1:\n\t" \
  "v_mul_f32 v214, v214, v88\n\t" \
  "v_mul_f32 v215, v215, v88\n\t" \
  "v_cvt_pk_bf16_f32 v214, v214, v215\n\t" \
  "ds_write_b16 v244, v214 offset:7456\n\t" \
  "ds_write_b16_d16_hi v244, v214 offset:7520\n\t" \
  "ds_read_b128 v[200:203], v245 offset:7456\n\t" \
  "ds_read_b128 v[204:207], v245 offset:7488\n\t" \
  "v_mov_b32 v214, v212\n\t" \
  "v_mov_b32 v215, v213\n\t" \
  "s_waitcnt lgkmcnt(10)\n\t" \
  "v_mfma_f32_32x32x16_bf16 v[116:131], v[92:95], v[180:183], v[116:131]\n\t" \
  "s_nop 0\n\t" \
  "v_rcp_f32 v132, v132\n\t" \
  "v_rcp_f32 v133, v133\n\t" \
  "v_rcp_f32 v134, v134\n\t" \
  "v_rcp_f32 v135, v135\n\t" \
  "v_mfma_f32_32x32x16_bf16 v[116:131], v[8:11], v[164:167], v[116:131]\n\t" \
  "v_rcp_f32 v136, v136\n\t" \
  "v_rcp_f32 v137, v137\n\t" \
  "v_rcp_f32 v138, v138\n\t" \
  "v_rcp_f32 v139, v139\n\t" \
  "v_mfma_f32_32x32x16_bf16 v[116:131], v[16:19], v[168:171], v[116:131]\n\t" \
  "v_mul_f32 v132, v100, v132\n\t" \
  "v_mul_f32 v133, v101, v133\n\t" \
  "v_mul_f32 v134, v102, v134\n\t" \
  "v_mul_f32 v135, v103, v135\n\t" \
  "v_cvt_pk_bf16_f32 v132, v132, v133\n\t" \
  "v_cvt_pk_bf16_f32 v133, v134, v135\n\t" \
  "v_rcp_f32 v140, v140\n\t" \
  "v_rcp_f32 v141, v141\n\t" \
  "v_rcp_f32 v142, v142\n\t" \
  "v_rcp_f32 v143, v143\n\t" \
  "v_mfma_f32_32x32x16_bf16 v[116:131], v[24:27], v[172:175], v[116:131]\n\t" \
  "v_mul_f32 v136, v104, v136\n\t" \
  "v_mul_f32 v137, v105, v137\n\t" \
  "v_mul_f32 v138, v106, v138\n\t" \
  "v_mul_f32 v139, v107, v139\n\t" \
  "v_cvt_pk_bf16_f32 v134, v136, v137\n\t" \
  "v_cvt_pk_bf16_f32 v135, v138, v139\n\t" \
  "v_mfma_f32_32x32x16_bf16 v[116:131], v[32:35], v[176:179], v[116:131]\n\t" \
  "v_rcp_f32 v144, v144\n\t" \
  "v_rcp_f32 v145, v145\n\t" \
  "v_rcp_f32 v146, v146\n\t" \
  "v_rcp_f32 v147, v147\n\t" \
  "s_waitcnt lgkmcnt(1)\n\t" \
  "v_mfma_f32_16x16x32_bf16 v[84:87], v[200:203], v[192:195], v[84:87]\n\t" \
  "v_mul_f32 v140, v108, v140\n\t" \
  "v_mul_f32 v141, v109, v141\n\t" \
  "v_mul_f32 v142, v110, v142\n\t" \
  "v_mul_f32 v143, v111, v143\n\t" \
  "v_cvt_pk_bf16_f32 v136, v140, v141\n\t" \
  "v_cvt_pk_bf16_f32 v137, v142, v143\n\t" \
  "s_waitcnt lgkmcnt(0)\n\t" \
  "v_mfma_f32_16x16x32_bf16 v[84:87], v[204:207], v[196:199], v[84:87]\n\t" \
  "ds_write_b128 v242, v[132:135] offset:0\n\t" \
  "ds_read_b128 v[92:95], v246 offset:2048\n\t" \
  "v_mul_f32 v144, v112, v144\n\t" \
  "v_mul_f32 v145, v113, v145\n\t" \
  "v_mul_f32 v146, v114, v146\n\t" \
  "v_mul_f32 v147, v115, v147\n\t" \
  "v_cvt_pk_bf16_f32 v138, v144, v145\n\t" \
  "v_cvt_pk_bf16_f32 v139, v146, v147\n\t" \
  "ds_write_b128 v242, v[136:139] offset:16\n\t" \
  "ds_read_b64_tr_b16 v[200:201], v243 offset:0\n\t" \
  "ds_read_b64_tr_b16 v[202:203], v243 offset:320\n\t" \
  "ds_read_b64_tr_b16 v[204:205], v243 offset:1280\n\t" \
  "ds_read_b64_tr_b16 v[206:207], v243 offset:1600\n\t" \
  "ds_read_b128 v[192:195], v241 offset:5376\n\t" \
  "ds_read_b128 v[196:199], v241 offset:5408\n\t" \
  "ds_write_b16 v229, v254 offset:5376\n\t" \
  "ds_read_b128 v[96:99], v246 offset:3072\n\t" \
  "s_sub_u32 %[nt], %[nt], 1\n\t" \
  "s_cmp_lg_u32 %[nt], %[thr]\n\t" \
  "s_cbranch_scc1 3f\n\t" \
  "s_setprio 0\n\t" \
  "3:\n\t" \
  "s_cmp_lt_i32 %[nt], 1\n\t" \
  "s_cselect_b64 vcc, -1, 0\n\t" \
  "v_exp_f32 v148, v116\n\t" \
  "v_exp_f32 v149, v117\n\t" \
  "v_exp_f32 v150, v118\n\t" \
  "v_exp_f32 v151, v119\n\t" \
  "s_waitcnt vmcnt(0)\n\t" \
  "ds_read_b128 v[164:167], v232\n\t" \
  "ds_read_b128 v[168:171], v233\n\t" \
  "ds_read_b128 v[172:175], v234\n\t" \
  "ds_read_b128 v[176:179], v235\n\t" \
  "s_waitcnt lgkmcnt(11)\n\t" \
  "ds_read_b128 v[180:183], v236 offset:4096\n\t" \
  "ds_read_u16_d16_hi v210, v238 offset:5120\n\t" \
  "ds_read_u16_d16_hi v211, v237 offset:5126\n\t" \
  "v_add_u32 v230, v247, v228\n\t" \
  "v_add_u32 v231, v239, v228\n\t" \
  "ds_read_b64 v[208:209], v230\n\t" \
  "s_waitcnt lgkmcnt(11)\n\t" \
  "ds_read_b128 v[188:191], v231 offset:32\n\t" \
  "ds_read_b128 v[184:187], v231\n\t" \
  "v_mfma_f32_16x16x32_bf16 v[88:91], v[92:95], v[132:135], 0\n\t" \
  "v_exp_f32 v152, v120\n\t" \
  "v_exp_f32 v153, v121\n\t" \
  "v_exp_f32 v154, v122\n\t" \
  "v_exp_f32 v155, v123\n\t" \
  "v_mfma_f32_32x32x16_bf16 v[52:67], v[200:203], v[192:195], v[52:67]\n\t" \
  "v_exp_f32 v156, v124\n\t" \
  "v_exp_f32 v157, v125\n\t" \
  "v_exp_f32 v158, v126\n\t" \
  "v_exp_f32 v159, v127\n\t" \
  "s_waitcnt lgkmcnt(10)\n\t" \
  "v_mfma_f32_16x16x32_bf16 v[88:91], v[96:99], v[136:139], v[88:91]\n\t" \
  "s_waitcnt lgkmcnt(3)\n\t" \
  "s_cbranch_vccnz 41f\n\t" \
  "s_add_u32 m0, %[b1], 4096\n\t" \
  "s_nop 0\n\t" \
  "buffer_load_dwordx4 v[224:225], %[rse], 0 idxen offen lds\n\t" \
  "buffer_load_dword v[226:227], %[rsp], 0 idxen offen offset:1024 lds\n\t" \
  "s_mov_b32 m0, %[b1]\n\t" \
  "s_nop 0\n\t" \
  "buffer_load_dwordx4 v[216:217], %[rsh], 0 idxen offen  lds\n\t" \
  "41:\n\t" \
  "ds_read_b128 v[96:99], v246 offset:0\n\t" \
  "v_exp_f32 v160, v128\n\t" \
  "v_exp_f32 v161, v129\n\t" \
  "v_exp_f32 v162, v130\n\t" \
  "v_exp_f32 v163, v131\n\t" \
  "v_mfma_f32_32x32x16_bf16 v[52:67], v[204:207], v[196:199], v[52:67]\n\t" \
  "v_add_u32 v229, v240, v228\n\t" \
  "ds_write_b16_d16_hi v229, v254 offset:5376\n\t" \
  "v_mfma_f32_32x32x8bf16_1k v[148:163], v[252:253], v[252:253], v[148:163]\n\t" \
  "s_waitcnt lgkmcnt(2)\n\t" \
  "v_mfma_f32_32x32x16_bf16 v[100:115], v[36:39], v[184:187], 0\n\t" \
  "ds_read_u16 v228, v250\n\t" \
  "v_add_u32 v250, 0x100, v250\n\t" \
  "v_sub_f32 v212, v208, v210\n\t" \
  "v_sub_f32 v213, v209, v211\n\t" \
  "v_mul_f32 v208, v212, v212\n\t" \
  "v_mul_f32 v209, v213, v213\n\t" \
  "v_cvt_pk_bf16_f32 v191, v209, v208\n\t" \
  "v_add_u32 v248, 0x100, v248\n\t" \
  "v_add_u32 v249, 0x100, v249\n\t" \
  "v_mfma_f32_32x32x16_bf16 v[100:115], v[4:7], v[164:167], v[100:115]\n\t" \
  "s_nop 0\n\t" \
  "v_rcp_f32 v148, v148\n\t" \
  "v_rcp_f32 v149, v149\n\t" \
  "v_rcp_f32 v150, v150\n\t" \
  "v_rcp_f32 v151, v151\n\t" \
  "v_mfma_f32_32x32x16_bf16 v[100:115], v[12:15], v[168:171], v[100:115]\n\t" \
  "v_rcp_f32 v152, v152\n\t" \
  "v_rcp_f32 v153, v153\n\t" \
  "v_rcp_f32 v154, v154\n\t" \
  "v_rcp_f32 v155, v155\n\t" \
  "v_mfma_f32_32x32x16_bf16 v[100:115], v[20:23], v[172:175], v[100:115]\n\t" \
  "v_mul_f32 v148, v116, v148\n\t" \
  "v_mul_f32 v149, v117, v149\n\t" \
  "v_mul_f32 v150, v118, v150\n\t" \
  "v_mul_f32 v151, v119, v151\n\t" \
  "v_cvt_pk_bf16_f32 v148, v148, v149\n\t" \
  "v_cvt_pk_bf16_f32 v149, v150, v151\n\t" \
  "s_cbranch_vccnz 42f\n\t" \
  "buffer_load_dwordx4 v[218:219], %[rsh], 0 idxen offen offset:1024 lds\n\t" \
  "buffer_load_dwordx4 v[220:221], %[rsh], 0 idxen offen offset:2048 lds\n\t" \
  "buffer_load_dwordx4 v[222:223], %[rsh], 0 idxen offen offset:3072 lds\n\t" \
  "42:\n\t" \
  "v_rcp_f32 v156, v156\n\t" \
  "v_rcp_f32 v157, v157\n\t" \
  "v_rcp_f32 v158, v158\n\t" \
  "v_rcp_f32 v159, v159\n\t" \
  "v_mfma_f32_32x32x16_bf16 v[100:115], v[28:31], v[176:179], v[100:115]\n\t" \
  "v_mul_f32 v152, v120, v152\n\t" \
  "v_mul_f32 v153, v121, v153\n\t" \
  "v_mul_f32 v154, v122, v154\n\t" \
  "v_mul_f32 v155, v123, v155\n\t" \
  "v_cvt_pk_bf16_f32 v150, v152, v153\n\t" \
  "v_cvt_pk_bf16_f32 v151, v154, v155\n\t" \
  "s_waitcnt lgkmcnt(2)\n\t" \
  "v_mfma_f32_32x32x16_bf16 v[100:115], v[96:99], v[180:183], v[100:115]\n\t" \
  "v_rcp_f32 v160, v160\n\t" \
  "v_rcp_f32 v161, v161\n\t" \
  "v_rcp_f32 v162, v162\n\t" \
  "v_rcp_f32 v163, v163\n\t" \
  "ds_write_b128 v242, v[148:151] offset:0\n\t" \
  "ds_read_b128 v[92:95], v246 offset:4096\n\t" \
  "v_mul_f32 v156, v124, v156\n\t" \
  "v_mul_f32 v157, v125, v157\n\t" \
  "v_mul_f32 v158, v126, v158\n\t" \
  "v_mul_f32 v159, v127, v159\n\t" \
  "v_cvt_pk_bf16_f32 v152, v156, v157\n\t" \
  "v_cvt_pk_bf16_f32 v153, v158, v159\n\t" \
  "v_mfma_f32_32x32x16_bf16 v[100:115], v[44:47], v[188:191], v[100:115]\n\t" \
  "v_mul_f32 v160, v128, v160\n\t" \
  "v_mul_f32 v161, v129, v161\n\t" \
  "v_mul_f32 v162, v130, v162\n\t" \
  "v_mul_f32 v163, v131, v163\n\t" \
  "v_cvt_pk_bf16_f32 v154, v160, v161\n\t" \
  "v_cvt_pk_bf16_f32 v155, v162, v163\n\t" \
  "s_cmp_eq_u32 %[nt], 0\n\t" \
  "s_cbranch_scc1 5f\n\t" \
  "ds_write_b128 v242, v[152:155] offset:16\n\t" \
  "ds_read_b64_tr_b16 v[200:201], v243 offset:0\n\t" \
  "ds_read_b64_tr_b16 v[202:203], v243 offset:320\n\t" \
  "ds_read_b64_tr_b16 v[204:205], v243 offset:1280\n\t" \
  "ds_read_b64_tr_b16 v[206:207], v243 offset:1600\n\t" \
  "ds_read_b128 v[96:99], v246 offset:5120\n\t" \
  "v_exp_f32 v132, v100\n\t" \
  "v_exp_f32 v133, v101\n\t" \
  "v_exp_f32 v134, v102\n\t" \
  "v_exp_f32 v135, v103\n\t" \
  "s_waitcnt lgkmcnt(6)\n\t" \
  "v_mfma_f32_16x16x32_bf16 v[88:91], v[92:95], v[148:151], v[88:91]\n\t" \
  "ds_read_b128 v[92:95], v246 offset:1024\n\t" \
  "v_exp_f32 v136, v104\n\t" \
  "v_exp_f32 v137, v105\n\t" \
  "v_exp_f32 v138, v106\n\t" \
  "v_exp_f32 v139, v107\n\t" \
  "s_waitcnt lgkmcnt(4)\n\t" \
  "v_mfma_f32_32x32x16_bf16 v[68:83], v[200:203], v[192:195], v[68:83]\n\t" \
  "ds_read_u16 v216, v248 offset:0\n\t" \
  "ds_read_u16 v218, v248 offset:8\n\t" \
  "ds_read_u16 v220, v248 offset:16\n\t" \
  "ds_read_u16 v222, v248 offset:24\n\t" \
  "ds_read_b32 v224, v249\n\t" \
  "ds_read_u16 v226, v249 offset:4\n\t" \
  "v_exp_f32 v140, v108\n\t" \
  "v_exp_f32 v141, v109\n\t" \
  "v_exp_f32 v142, v110\n\t" \
  "v_exp_f32 v143, v111\n\t" \
  "s_waitcnt lgkmcnt(7)\n\t" \
  "v_mfma_f32_16x16x32_bf16 v[88:91], v[96:99], v[152:155], v[88:91]\n\t" \
  "v_mfma_f32_32x32x16_bf16 v[116:131], v[40:43], v[184:187], 0\n\t" \
  "v_exp_f32 v144, v112\n\t" \
  "v_exp_f32 v145, v113\n\t" \
  "v_exp_f32 v146, v114\n\t" \
  "v_exp_f32 v147, v115\n\t" \
  "v_mfma_f32_32x32x16_bf16 v[68:83], v[204:207], v[196:199], v[68:83]\n\t" \
  "s_nop 0\n\t" \
  "v_mfma_f32_32x32x8bf16_1k v[132:147], v[252:253], v[252:253], v[132:147]\n\t" \
  "v_mfma_f32_32x32x16_bf16 v[116:131], v[48:51], v[188:191], v[116:131]\n\t" \
  "s_branch 1b\n\t" \
  "5:\n\t" \
  "s_waitcnt lgkmcnt(0)\n\t" \
  "s_cmp_lg_u32 %[last], 0\n\t" \
  "s_cbranch_scc1 8f\n\t" \
  "ds_write_b128 v242, v[152:155] offset:16\n\t" \
  "ds_read_b64_tr_b16 v[200:201], v243 offset:0\n\t" \
  "ds_read_b64_tr_b16 v[202:203], v243 offset:320\n\t" \
  "ds_read_b64_tr_b16 v[204:205], v243 offset:1280\n\t" \
  "ds_read_b64_tr_b16 v[206:207], v243 offset:1600\n\t" \
  "ds_read_b128 v[96:99], v246 offset:5120\n\t" \
  "v_mfma_f32_16x16x32_bf16 v[88:91], v[92:95], v[148:151], v[88:91]\n\t" \
  "s_waitcnt lgkmcnt(3)\n\t" \
  "v_mfma_f32_32x32x16_bf16 v[68:83], v[200:203], v[192:195], v[68:83]\n\t" \
  "s_waitcnt lgkmcnt(0)\n\t" \
  "v_mfma_f32_16x16x32_bf16 v[88:91], v[96:99], v[152:155], v[88:91]\n\t" \
  "v_mfma_f32_32x32x16_bf16 v[68:83], v[204:207], v[196:199], v[68:83]\n\t" \
  "s_nop 6\n\t" \
  "v_mul_f32 v214, v214, v88\n\t" \
  "v_mul_f32 v215, v215, v88\n\t" \
  "v_cvt_pk_bf16_f32 v214, v214, v215\n\t" \
  "ds_write_b16 v244, v214 offset:7456\n\t" \
  "ds_write_b16_d16_hi v244, v214 offset:7520\n\t" \
  "ds_read_b128 v[200:203], v245 offset:7456\n\t" \
  "ds_read_b128 v[204:207], v245 offset:7488\n\t" \
  "s_waitcnt lgkmcnt(1)\n\t" \
  "v_mfma_f32_16x16x32_bf16 v[84:87], v[200:203], v[192:195], v[84:87]\n\t" \
  "s_waitcnt lgkmcnt(0)\n\t" \
  "v_mfma_f32_16x16x32_bf16 v[84:87], v[204:207], v[196:199], v[84:87]\n\t" \
  "s_branch 9f\n\t" \
  "6:\n\t" \
  "s_cmp_lg_u32 %[last], 0\n\t" \
  "s_cbranch_scc0 9f\n\t" \
  "s_waitcnt lgkmcnt(0)\n\t" \
  "v_cmp_eq_u32 vcc, 0, v252\n\t" \
  "v_cndmask_b32 v228, 0, 24, vcc\n\t" \
  "v_xor_b32 v216, v232, v228\n\t" \
  "v_xor_b32 v217, v233, v228\n\t" \
  "v_xor_b32 v218, v234, v228\n\t" \
  "v_xor_b32 v219, v235, v228\n\t" \
  "v_xor_b32 v220, 16, v216\n\t" \
  "v_xor_b32 v221, 16, v217\n\t" \
  "v_xor_b32 v222, 16, v218\n\t" \
  "v_xor_b32 v223, 16, v219\n\t" \
  "ds_read_b64 v[164:165], v216\n\t" \
  "ds_read_b64 v[166:167], v220\n\t" \
  "ds_read_b64 v[168:169], v217\n\t" \
  "ds_read_b64 v[170:171], v221\n\t" \
  "ds_read_b64 v[172:173], v218\n\t" \
  "ds_read_b64 v[174:175], v222\n\t" \
  "ds_read_b64 v[176:177], v219\n\t" \
  "ds_read_b64 v[178:179], v223\n\t" \
  "ds_read_b128 v[4:7], v246 offset:6144\n\t" \
  "ds_read_b128 v[8:11], v246 offset:7168\n\t" \
  "ds_read_b128 v[12:15], v246 offset:8192\n\t" \
  "ds_read_b128 v[16:19], v246 offset:9216\n\t" \
  "ds_read_b128 v[20:23], v246 offset:10240\n\t" \
  "ds_read_b128 v[24:27], v246 offset:11264\n\t" \
  "v_subrev_u32 v224, %[b1], v237\n\t" \
  "v_lshl_add_u32 v224, v224, 3, v242\n\t" \
  "v_sub_u32 v224, v224, v228\n\t" \
  "s_nop 4\n\t" \
  "v_cvt_pk_bf16_f32 v132, v52, v53\n\t" \
  "v_cvt_pk_bf16_f32 v133, v54, v55\n\t" \
  "v_cvt_pk_bf16_f32 v134, v56, v57\n\t" \
  "v_cvt_pk_bf16_f32 v135, v58, v59\n\t" \
  "v_cvt_pk_bf16_f32 v136, v60, v61\n\t" \
  "v_cvt_pk_bf16_f32 v137, v62, v63\n\t" \
  "v_cvt_pk_bf16_f32 v138, v64, v65\n\t" \
  "v_cvt_pk_bf16_f32 v139, v66, v67\n\t" \
  "v_cvt_pk_bf16_f32 v36, v68, v69\n\t" \
  "v_cvt_pk_bf16_f32 v37, v70, v71\n\t" \
  "v_cvt_pk_bf16_f32 v38, v72, v73\n\t" \
  "v_cvt_pk_bf16_f32 v39, v74, v75\n\t" \
  "v_cvt_pk_bf16_f32 v40, v76, v77\n\t" \
  "v_cvt_pk_bf16_f32 v41, v78, v79\n\t" \
  "v_cvt_pk_bf16_f32 v42, v80, v81\n\t" \
  "v_cvt_pk_bf16_f32 v43, v82, v83\n\t" \
  "s_waitcnt lgkmcnt(5)\n\t" \
  "v_mfma_f32_32x32x16_bf16 v[100:115], v[4:7], v[164:167], 0\n\t" \
  "s_waitcnt lgkmcnt(4)\n\t" \
  "v_mfma_f32_32x32x16_bf16 v[116:131], v[8:11], v[164:167], 0\n\t" \
  "ds_read_b128 v[28:31], v246 offset:12288\n\t" \
  "ds_read_b128 v[32:35], v246 offset:13312\n\t" \
  "s_waitcnt lgkmcnt(5)\n\t" \
  "v_mfma_f32_32x32x16_bf16 v[100:115], v[12:15], v[168:171], v[100:115]\n\t" \
  "s_waitcnt lgkmcnt(4)\n\t" \
  "v_mfma_f32_32x32x16_bf16 v[116:131], v[16:19], v[168:171], v[116:131]\n\t" \
  "ds_read_b128 v[4:7], v246 offset:14336\n\t" \
  "ds_read_b128 v[8:11], v246 offset:15360\n\t" \
  "s_waitcnt lgkmcnt(5)\n\t" \
  "v_mfma_f32_32x32x16_bf16 v[100:115], v[20:23], v[172:175], v[100:115]\n\t" \
  "s_waitcnt lgkmcnt(4)\n\t" \
  "v_mfma_f32_32x32x16_bf16 v[116:131], v[24:27], v[172:175], v[116:131]\n\t" \
  "ds_read_b128 v[12:15], v246 offset:16384\n\t" \
  "ds_read_b128 v[16:19], v246 offset:17408\n\t" \
  "s_waitcnt lgkmcnt(5)\n\t" \
  "v_mfma_f32_32x32x16_bf16 v[100:115], v[28:31], v[176:179], v[100:115]\n\t" \
  "s_waitcnt lgkmcnt(4)\n\t" \
  "v_mfma_f32_32x32x16_bf16 v[116:131], v[32:35], v[176:179], v[116:131]\n\t" \
  "ds_read_b128 v[20:23], v246 offset:18432\n\t" \
  "ds_read_b128 v[24:27], v246 offset:19456\n\t" \
  "s_waitcnt lgkmcnt(5)\n\t" \
  "v_mfma_f32_32x32x16_bf16 v[100:115], v[4:7], v[132:135], v[100:115]\n\t" \
  "ds_read_b128 v[28:31], v246 offset:20480\n\t" \
  "ds_read_b128 v[32:35], v246 offset:21504\n\t" \
  "s_waitcnt lgkmcnt(5)\n\t" \
  "v_mfma_f32_32x32x16_bf16 v[100:115], v[12:15], v[136:139], v[100:115]\n\t" \
  "s_waitcnt lgkmcnt(3)\n\t" \
  "v_mfma_f32_32x32x16_bf16 v[100:115], v[20:23], v[36:39], v[100:115]\n\t" \
  "s_waitcnt lgkmcnt(1)\n\t" \
  "v_mfma_f32_32x32x16_bf16 v[100:115], v[28:31], v[40:43], v[100:115]\n\t" \
  "s_waitcnt lgkmcnt(0)\n\t" \
  "s_branch 10f\n\t" \
  "8:\n\t" \
  "ds_write_b128 v242, v[152:155] offset:16\n\t" \
  "ds_read_b64_tr_b16 v[200:201], v243 offset:0\n\t" \
  "ds_read_b64_tr_b16 v[202:203], v243 offset:320\n\t" \
  "ds_read_b64_tr_b16 v[204:205], v243 offset:1280\n\t" \
  "ds_read_b64_tr_b16 v[206:207], v243 offset:1600\n\t" \
  "ds_read_b128 v[96:99], v246 offset:5120\n\t" \
  "v_mfma_f32_16x16x32_bf16 v[88:91], v[92:95], v[148:151], v[88:91]\n\t" \
  "s_waitcnt lgkmcnt(0)\n\t" \
  "v_mfma_f32_16x16x32_bf16 v[88:91], v[96:99], v[152:155], v[88:91]\n\t" \
  "v_cmp_eq_u32 vcc, 0, v252\n\t" \
  "v_cndmask_b32 v228, 0, 24, vcc\n\t" \
  "v_xor_b32 v216, v232, v228\n\t" \
  "v_xor_b32 v217, v233, v228\n\t" \
  "v_xor_b32 v218, v234, v228\n\t" \
  "v_xor_b32 v219, v235, v228\n\t" \
  "v_xor_b32 v220, 16, v216\n\t" \
  "v_xor_b32 v221, 16, v217\n\t" \
  "v_xor_b32 v222, 16, v218\n\t" \
  "v_xor_b32 v223, 16, v219\n\t" \
  "ds_read_b64 v[164:165], v216\n\t" \
  "ds_read_b64 v[166:167], v220\n\t" \
  "ds_read_b64 v[168:169], v217\n\t" \
  "ds_read_b64 v[170:171], v221\n\t" \
  "ds_read_b64 v[172:173], v218\n\t" \
  "ds_read_b64 v[174:175], v222\n\t" \
  "ds_read_b64 v[176:177], v219\n\t" \
  "ds_read_b64 v[178:179], v223\n\t" \
  "ds_read_b128 v[4:7], v246 offset:6144\n\t" \
  "ds_read_b128 v[8:11], v246 offset:7168\n\t" \
  "ds_read_b128 v[12:15], v246 offset:8192\n\t" \
  "ds_read_b128 v[16:19], v246 offset:9216\n\t" \
  "ds_read_b128 v[20:23], v246 offset:10240\n\t" \
  "ds_read_b128 v[24:27], v246 offset:11264\n\t" \
  "v_subrev_u32 v224, %[b1], v237\n\t" \
  "v_lshl_add_u32 v224, v224, 3, v242\n\t" \
  "v_sub_u32 v224, v224, v228\n\t" \
  "v_mfma_f32_32x32x16_bf16 v[68:83], v[200:203], v[192:195], v[68:83]\n\t" \
  "s_waitcnt lgkmcnt(5)\n\t" \
  "v_mfma_f32_32x32x16_bf16 v[100:115], v[4:7], v[164:167], 0\n\t" \
  "v_mfma_f32_32x32x16_bf16 v[68:83], v[204:207], v[196:199], v[68:83]\n\t" \
  "s_waitcnt lgkmcnt(4)\n\t" \
  "v_mfma_f32_32x32x16_bf16 v[116:131], v[8:11], v[164:167], 0\n\t" \
  "ds_read_b128 v[28:31], v246 offset:12288\n\t" \
  "ds_read_b128 v[32:35], v246 offset:13312\n\t" \
  "s_waitcnt lgkmcnt(5)\n\t" \
  "v_mfma_f32_32x32x16_bf16 v[100:115], v[12:15], v[168:171], v[100:115]\n\t" \
  "v_mul_f32 v214, v214, v88\n\t" \
  "v_mul_f32 v215, v215, v88\n\t" \
  "v_cvt_pk_bf16_f32 v214, v214, v215\n\t" \
  "ds_write_b16 v244, v214 offset:7456\n\t" \
  "ds_write_b16_d16_hi v244, v214 offset:7520\n\t" \
  "ds_read_b128 v[200:203], v245 offset:7456\n\t" \
  "ds_read_b128 v[204:207], v245 offset:7488\n\t" \
  "v_cvt_pk_bf16_f32 v132, v52, v53\n\t" \
  "v_cvt_pk_bf16_f32 v133, v54, v55\n\t" \
  "v_cvt_pk_bf16_f32 v134, v56, v57\n\t" \
  "v_cvt_pk_bf16_f32 v135, v58, v59\n\t" \
  "s_waitcnt lgkmcnt(8)\n\t" \
  "v_mfma_f32_32x32x16_bf16 v[116:131], v[16:19], v[168:171], v[116:131]\n\t" \
  "ds_read_b128 v[4:7], v246 offset:14336\n\t" \
  "ds_read_b128 v[8:11], v246 offset:15360\n\t" \
  "s_waitcnt lgkmcnt(9)\n\t" \
  "v_mfma_f32_32x32x16_bf16 v[100:115], v[20:23], v[172:175], v[100:115]\n\t" \
  "v_cvt_pk_bf16_f32 v136, v60, v61\n\t" \
  "v_cvt_pk_bf16_f32 v137, v62, v63\n\t" \
  "v_cvt_pk_bf16_f32 v138, v64, v65\n\t" \
  "v_cvt_pk_bf16_f32 v139, v66, v67\n\t" \
  "s_waitcnt lgkmcnt(8)\n\t" \
  "v_mfma_f32_32x32x16_bf16 v[116:131], v[24:27], v[172:175], v[116:131]\n\t" \
  "ds_read_b128 v[12:15], v246 offset:16384\n\t" \
  "ds_read_b128 v[16:19], v246 offset:17408\n\t" \
  "s_waitcnt lgkmcnt(9)\n\t" \
  "v_mfma_f32_32x32x16_bf16 v[100:115], v[28:31], v[176:179], v[100:115]\n\t" \
  "v_cvt_pk_bf16_f32 v36, v68, v69\n\t" \
  "v_cvt_pk_bf16_f32 v37, v70, v71\n\t" \
  "v_cvt_pk_bf16_f32 v38, v72, v73\n\t" \
  "v_cvt_pk_bf16_f32 v39, v74, v75\n\t" \
  "s_waitcnt lgkmcnt(5)\n\t" \
  "v_mfma_f32_16x16x32_bf16 v[84:87], v[200:203], v[192:195], v[84:87]\n\t" \
  "v_cvt_pk_bf16_f32 v40, v76, v77\n\t" \
  "v_cvt_pk_bf16_f32 v41, v78, v79\n\t" \
  "v_cvt_pk_bf16_f32 v42, v80, v81\n\t" \
  "v_cvt_pk_bf16_f32 v43, v82, v83\n\t" \
  "v_mfma_f32_32x32x16_bf16 v[116:131], v[32:35], v[176:179], v[116:131]\n\t" \
  "ds_read_b128 v[20:23], v246 offset:18432\n\t" \
  "ds_read_b128 v[24:27], v246 offset:19456\n\t" \
  "s_waitcnt lgkmcnt(6)\n\t" \
  "v_mfma_f32_16x16x32_bf16 v[84:87], v[204:207], v[196:199], v[84:87]\n\t" \
  "s_waitcnt lgkmcnt(5)\n\t" \
  "v_mfma_f32_32x32x16_bf16 v[100:115], v[4:7], v[132:135], v[100:115]\n\t" \
  "ds_read_b128 v[28:31], v246 offset:20480\n\t" \
  "ds_read_b128 v[32:35], v246 offset:21504\n\t" \
  "s_waitcnt lgkmcnt(5)\n\t" \
  "v_mfma_f32_32x32x16_bf16 v[100:115], v[12:15], v[136:139], v[100:115]\n\t" \
  "s_waitcnt lgkmcnt(3)\n\t" \
  "v_mfma_f32_32x32x16_bf16 v[100:115], v[20:23], v[36:39], v[100:115]\n\t" \
  "s_waitcnt lgkmcnt(1)\n\t" \
  "v_mfma_f32_32x32x16_bf16 v[100:115], v[28:31], v[40:43], v[100:115]\n\t" \
  "s_waitcnt lgkmcnt(0)\n\t" \
  "10:\n\t" \
  "v_mov_b32 v156, 0xbfb8aa3b\n\t" \
  "v_mfma_f32_32x32x16_bf16 v[116:131], v[8:11], v[132:135], v[116:131]\n\t" \
  "v_mfma_f32_32x32x16_bf16 v[116:131], v[16:19], v[136:139], v[116:131]\n\t" \
  "s_nop 8\n\t" \
  "v_exp_f32 v148, v100\n\t" \
  "v_exp_f32 v149, v101\n\t" \
  "v_exp_f32 v150, v102\n\t" \
  "v_exp_f32 v151, v103\n\t" \
  "v_lshlrev_b32 v200, 16, v164\n\t" \
  "v_and_b32 v201, 0xffff0000, v164\n\t" \
  "v_lshlrev_b32 v202, 16, v165\n\t" \
  "v_and_b32 v203, 0xffff0000, v165\n\t" \
  "v_mfma_f32_32x32x16_bf16 v[116:131], v[24:27], v[36:39], v[116:131]\n\t" \
  "v_fmaak_f32 v148, v148, v156, 0xbfb8aa3b\n\t" \
  "v_fmaak_f32 v149, v149, v156, 0xbfb8aa3b\n\t" \
  "v_fmaak_f32 v150, v150, v156, 0xbfb8aa3b\n\t" \
  "v_fmaak_f32 v151, v151, v156, 0xbfb8aa3b\n\t" \
  "v_rcp_f32 v148, v148\n\t" \
  "v_rcp_f32 v149, v149\n\t" \
  "v_rcp_f32 v150, v150\n\t" \
  "v_rcp_f32 v151, v151\n\t" \
  "v_mfma_f32_32x32x16_bf16 v[116:131], v[32:35], v[40:43], v[116:131]\n\t" \
  "v_exp_f32 v152, v104\n\t" \
  "v_exp_f32 v153, v105\n\t" \
  "v_exp_f32 v154, v106\n\t" \
  "v_exp_f32 v155, v107\n\t" \
  "v_fmac_f32 v200, v100, v148\n\t" \
  "v_fmac_f32 v201, v101, v149\n\t" \
  "v_fmac_f32 v202, v102, v150\n\t" \
  "v_fmac_f32 v203, v103, v151\n\t" \
  "v_lshlrev_b32 v204, 16, v166\n\t" \
  "v_and_b32 v205, 0xffff0000, v166\n\t" \
  "v_lshlrev_b32 v206, 16, v167\n\t" \
  "v_and_b32 v207, 0xffff0000, v167\n\t" \
  "v_fmaak_f32 v152, v152, v156, 0xbfb8aa3b\n\t" \
  "v_fmaak_f32 v153, v153, v156, 0xbfb8aa3b\n\t" \
  "v_fmaak_f32 v154, v154, v156, 0xbfb8aa3b\n\t" \
  "v_fmaak_f32 v155, v155, v156, 0xbfb8aa3b\n\t" \
  "v_cvt_pk_bf16_f32 v192, v200, v201\n\t" \
  "v_cvt_pk_bf16_f32 v193, v202, v203\n\t" \
  "ds_write_b64 v224, v[192:193] offset:0\n\t" \
  "v_rcp_f32 v152, v152\n\t" \
  "v_rcp_f32 v153, v153\n\t" \
  "v_rcp_f32 v154, v154\n\t" \
  "v_rcp_f32 v155, v155\n\t" \
  "v_exp_f32 v148, v108\n\t" \
  "v_exp_f32 v149, v109\n\t" \
  "v_exp_f32 v150, v110\n\t" \
  "v_exp_f32 v151, v111\n\t" \
  "v_fmac_f32 v204, v104, v152\n\t" \
  "v_fmac_f32 v205, v105, v153\n\t" \
  "v_fmac_f32 v206, v106, v154\n\t" \
  "v_fmac_f32 v207, v107, v155\n\t" \
  "v_lshlrev_b32 v200, 16, v168\n\t" \
  "v_and_b32 v201, 0xffff0000, v168\n\t" \
  "v_lshlrev_b32 v202, 16, v169\n\t" \
  "v_and_b32 v203, 0xffff0000, v169\n\t" \
  "v_fmaak_f32 v148, v148, v156, 0xbfb8aa3b\n\t" \
  "v_fmaak_f32 v149, v149, v156, 0xbfb8aa3b\n\t" \
  "v_fmaak_f32 v150, v150, v156, 0xbfb8aa3b\n\t" \
  "v_fmaak_f32 v151, v151, v156, 0xbfb8aa3b\n\t" \
  "v_cvt_pk_bf16_f32 v194, v204, v205\n\t" \
  "v_cvt_pk_bf16_f32 v195, v206, v207\n\t" \
  "ds_write_b64 v224, v[194:195] offset:16\n\t" \
  "v_rcp_f32 v148, v148\n\t" \
  "v_rcp_f32 v149, v149\n\t" \
  "v_rcp_f32 v150, v150\n\t" \
  "v_rcp_f32 v151, v151\n\t" \
  "v_exp_f32 v152, v112\n\t" \
  "v_exp_f32 v153, v113\n\t" \
  "v_exp_f32 v154, v114\n\t" \
  "v_exp_f32 v155, v115\n\t" \
  "v_fmac_f32 v200, v108, v148\n\t" \
  "v_fmac_f32 v201, v109, v149\n\t" \
  "v_fmac_f32 v202, v110, v150\n\t" \
  "v_fmac_f32 v203, v111, v151\n\t" \
  "v_lshlrev_b32 v204, 16, v170\n\t" \
  "v_and_b32 v205, 0xffff0000, v170\n\t" \
  "v_lshlrev_b32 v206, 16, v171\n\t" \
  "v_and_b32 v207, 0xffff0000, v171\n\t" \
  "v_fmaak_f32 v152, v152, v156, 0xbfb8aa3b\n\t" \
  "v_fmaak_f32 v153, v153, v156, 0xbfb8aa3b\n\t" \
  "v_fmaak_f32 v154, v154, v156, 0xbfb8aa3b\n\t" \
  "v_fmaak_f32 v155, v155, v156, 0xbfb8aa3b\n\t" \
  "v_cvt_pk_bf16_f32 v196, v200, v201\n\t" \
  "v_cvt_pk_bf16_f32 v197, v202, v203\n\t" \
  "ds_write_b64 v224, v[196:197] offset:32\n\t" \
  "v_rcp_f32 v152, v152\n\t" \
  "v_rcp_f32 v153, v153\n\t" \
  "v_rcp_f32 v154, v154\n\t" \
  "v_rcp_f32 v155, v155\n\t" \
  "v_exp_f32 v148, v116\n\t" \
  "v_exp_f32 v149, v117\n\t" \
  "v_exp_f32 v150, v118\n\t" \
  "v_exp_f32 v151, v119\n\t" \
  "v_fmac_f32 v204, v112, v152\n\t" \
  "v_fmac_f32 v205, v113, v153\n\t" \
  "v_fmac_f32 v206, v114, v154\n\t" \
  "v_fmac_f32 v207, v115, v155\n\t" \
  "v_lshlrev_b32 v200, 16, v172\n\t" \
  "v_and_b32 v201, 0xffff0000, v172\n\t" \
  "v_lshlrev_b32 v202, 16, v173\n\t" \
  "v_and_b32 v203, 0xffff0000, v173\n\t" \
  "v_fmaak_f32 v148, v148, v156, 0xbfb8aa3b\n\t" \
  "v_fmaak_f32 v149, v149, v156, 0xbfb8aa3b\n\t" \
  "v_fmaak_f32 v150, v150, v156, 0xbfb8aa3b\n\t" \
  "v_fmaak_f32 v151, v151, v156, 0xbfb8aa3b\n\t" \
  "v_cvt_pk_bf16_f32 v198, v204, v205\n\t" \
  "v_cvt_pk_bf16_f32 v199, v206, v207\n\t" \
  "ds_write_b64 v224, v[198:199] offset:48\n\t" \
  "v_rcp_f32 v148, v148\n\t" \
  "v_rcp_f32 v149, v149\n\t" \
  "v_rcp_f32 v150, v150\n\t" \
  "v_rcp_f32 v151, v151\n\t" \
  "v_exp_f32 v152, v120\n\t" \
  "v_exp_f32 v153, v121\n\t" \
  "v_exp_f32 v154, v122\n\t" \
  "v_exp_f32 v155, v123\n\t" \
  "v_fmac_f32 v200, v116, v148\n\t" \
  "v_fmac_f32 v201, v117, v149\n\t" \
  "v_fmac_f32 v202, v118, v150\n\t" \
  "v_fmac_f32 v203, v119, v151\n\t" \
  "v_lshlrev_b32 v204, 16, v174\n\t" \
  "v_and_b32 v205, 0xffff0000, v174\n\t" \
  "v_lshlrev_b32 v206, 16, v175\n\t" \
  "v_and_b32 v207, 0xffff0000, v175\n\t" \
  "v_fmaak_f32 v152, v152, v156, 0xbfb8aa3b\n\t" \
  "v_fmaak_f32 v153, v153, v156, 0xbfb8aa3b\n\t" \
  "v_fmaak_f32 v154, v154, v156, 0xbfb8aa3b\n\t" \
  "v_fmaak_f32 v155, v155, v156, 0xbfb8aa3b\n\t" \
  "v_cvt_pk_bf16_f32 v192, v200, v201\n\t" \
  "v_cvt_pk_bf16_f32 v193, v202, v203\n\t" \
  "ds_write_b64 v224, v[192:193] offset:64\n\t" \
  "v_rcp_f32 v152, v152\n\t" \
  "v_rcp_f32 v153, v153\n\t" \
  "v_rcp_f32 v154, v154\n\t" \
  "v_rcp_f32 v155, v155\n\t" \
  "v_exp_f32 v148, v124\n\t" \
  "v_exp_f32 v149, v125\n\t" \
  "v_exp_f32 v150, v126\n\t" \
  "v_exp_f32 v151, v127\n\t" \
  "v_fmac_f32 v204, v120, v152\n\t" \
  "v_fmac_f32 v205, v121, v153\n\t" \
  "v_fmac_f32 v206, v122, v154\n\t" \
  "v_fmac_f32 v207, v123, v155\n\t" \
  "v_lshlrev_b32 v200, 16, v176\n\t" \
  "v_and_b32 v201, 0xffff0000, v176\n\t" \
  "v_lshlrev_b32 v202, 16, v177\n\t" \
  "v_and_b32 v203, 0xffff0000, v177\n\t" \
  "v_fmaak_f32 v148, v148, v156, 0xbfb8aa3b\n\t" \
  "v_fmaak_f32 v149, v149, v156, 0xbfb8aa3b\n\t" \
  "v_fmaak_f32 v150, v150, v156, 0xbfb8aa3b\n\t" \
  "v_fmaak_f32 v151, v151, v156, 0xbfb8aa3b\n\t" \
  "v_cvt_pk_bf16_f32 v194, v204, v205\n\t" \
  "v_cvt_pk_bf16_f32 v195, v206, v207\n\t" \
  "ds_write_b64 v224, v[194:195] offset:80\n\t" \
  "v_rcp_f32 v148, v148\n\t" \
  "v_rcp_f32 v149, v149\n\t" \
  "v_rcp_f32 v150, v150\n\t" \
  "v_rcp_f32 v151, v151\n\t" \
  "v_exp_f32 v152, v128\n\t" \
  "v_exp_f32 v153, v129\n\t" \
  "v_exp_f32 v154, v130\n\t" \
  "v_exp_f32 v155, v131\n\t" \
  "v_fmac_f32 v200, v124, v148\n\t" \
  "v_fmac_f32 v201, v125, v149\n\t" \
  "v_fmac_f32 v202, v126, v150\n\t" \
  "v_fmac_f32 v203, v127, v151\n\t" \
  "v_lshlrev_b32 v204, 16, v178\n\t" \
  "v_and_b32 v205, 0xffff0000, v178\n\t" \
  "v_lshlrev_b32 v206, 16, v179\n\t" \
  "v_and_b32 v207, 0xffff0000, v179\n\t" \
  "v_fmaak_f32 v152, v152, v156, 0xbfb8aa3b\n\t" \
  "v_fmaak_f32 v153, v153, v156, 0xbfb8aa3b\n\t" \
  "v_fmaak_f32 v154, v154, v156, 0xbfb8aa3b\n\t" \
  "v_fmaak_f32 v155, v155, v156, 0xbfb8aa3b\n\t" \
  "v_cvt_pk_bf16_f32 v196, v200, v201\n\t" \
  "v_cvt_pk_bf16_f32 v197, v202, v203\n\t" \
  "ds_write_b64 v224, v[196:197] offset:96\n\t" \
  "v_rcp_f32 v152, v152\n\t" \
  "v_rcp_f32 v153, v153\n\t" \
  "v_rcp_f32 v154, v154\n\t" \
  "v_rcp_f32 v155, v155\n\t" \
  "s_nop 0\n\t" \
  "v_fmac_f32 v204, v128, v152\n\t" \
  "v_fmac_f32 v205, v129, v153\n\t" \
  "v_fmac_f32 v206, v130, v154\n\t" \
  "v_fmac_f32 v207, v131, v155\n\t" \
  "v_cvt_pk_bf16_f32 v198, v204, v205\n\t" \
  "v_cvt_pk_bf16_f32 v199, v206, v207\n\t" \
  "ds_write_b64 v224, v[198:199] offset:112\n\t" \
  "9:\n\t" \
  "ds_write_b16 v229, v254 offset:5376\n\t" \
  "s_mov_b32 m0, %[keep]\n\t" \
  "s_setprio 0\n\t" \
  "s_nop 11\n\t" \
  ""

DI u32x4 tr_frag(unsigned p, int half) {
  const s16x4 lo = __builtin_amdgcn_ds_read_tr16_b64_v4i16((lds_s16x4_p)p);
  const s16x4 hi = __builtin_amdgcn_ds_read_tr16_b64_v4i16((lds_s16x4_p)(p + half));
  return __builtin_bit_cast(u32x4, __builtin_shufflevector(lo, hi, 0, 1, 2, 3, 4, 5, 6, 7));
}
DI u32x4 fold(u32x4 v) {
#pragma unroll
  for (int i = 0; i < 4; i++) v[i] = pk_bf16(bf2f(v[i] & 0xffffu) * -LOG2E, __uint_as_float(v[i] & 0xffff0000u) * -LOG2E);
  return v;
}

__global__ __launch_bounds__(512, 2) void egnn_kernel(const char* __restrict__ hmat, const char* __restrict__ pos,
                                                      const char* __restrict__ ea, const int* __restrict__ src,
                                                      const int* __restrict__ dst, const char* __restrict__ we,
                                                      const char* __restrict__ wx, const char* __restrict__ wh,
                                                      char* __restrict__ ws, uint16_t* __restrict__ out, int N, int E STAMP_ARG) {
  const int w = __builtin_amdgcn_readfirstlane(threadIdx.x >> 6);
  const int bkt = (blockIdx.x & 7) * 32 + (blockIdx.x >> 3);
  const int n0 = bkt * NB;
  STAMP(0);
  unsigned wl = L_WAVE + w * WSZ, mi = L_MI + w * MISZ;
  const auto rs_tab = __builtin_amdgcn_make_buffer_rsrc((void*)(ws + WS_TAB), 0, NBK * NBK * 8, 0x00020000);
  const auto rs_rec = __builtin_amdgcn_make_buffer_rsrc((void*)(ws + WS_REC), 0, NBK * CH * 8, 0x00020000);
  const auto rs_h = __builtin_amdgcn_make_buffer_rsrc((void*)hmat, 0, N * 128, 0x00020000);
  const auto rs_ea = __builtin_amdgcn_make_buffer_rsrc((void*)ea, 0, E * 32, 0x00020000);
  unsigned tag_lo, tag_hi;

  {
    const int lane = lane_id(), tid = w * 64 + lane, r = lane & 31, h = lane >> 5;
    const int e0 = bkt * CH;
    int ne = max(E - e0, 0);
    asm("" : "+s"(ne));
    ne = min(ne, CH);
    const auto rs_d = __builtin_amdgcn_make_buffer_rsrc((void*)(dst + e0), 0, ne * 4, 0x00020000);
    const auto rs_s = __builtin_amdgcn_make_buffer_rsrc((void*)(src + e0), 0, ne * 4, 0x00020000);
    unsigned d[7], sv[7], wdv0, wdv1;
    u32x2 ep, xa, xb;
#pragma unroll
    for (int i = 0; i < 7; i++)
      asm volatile("buffer_load_dword %0, %2, %3, %5 offen nt\n\tbuffer_load_dword %1, %2, %4, %5 offen nt"
                   : "=&v"(d[i]), "=&v"(sv[i]) : "v"(tid * 4), "s"(rs_d), "s"(rs_s), "s"(i * 2048) : "memory");
    LDS(unsigned, L_CNT + tid * 4) = 0;
    barrier_lds();
    asm volatile("global_load_dwordx2 %0, %5, %6 sc1\n\tglobal_load_ushort %1, %7, %8\n\tglobal_load_ushort %2, %7, %8 offset:64\n\t"
                 "global_load_dwordx2 %3, %9, %10\n\tglobal_load_dwordx2 %4, %9, %10 offset:16"
                 : "=&v"(ep), "=&v"(wdv0), "=&v"(wdv1), "=&v"(xa), "=&v"(xb)
                 : "v"(0), "s"(ws + WS_EPOCH), "v"(r * 2), "s"(we + 128 * 128), "v"(h * 8), "s"(wx + ((w + 2) & 3) * 32) : "memory");
    {
      const int j = lane >> 3, jl = (j & 3) * 128 + (lane & 7) * 16, jh = (j >> 2) * 128;
      const char* const h0 = wh + (w < 4 ? 16 * w : 64 + 32 * ((w - 4) >> 1) + 4 * (w & 1)) * 128;
      const char* const e0p = we + (w < 4 ? 64 + 16 * w : 16 * (w - 4)) * 128;
      const unsigned oh = jl + jh * (w < 4 ? 8 : 16), oe = jl + jh * (w < 4 ? 4 : 8);
      const unsigned cc0 = ((lane & 7) ^ (lane >> 4)) * 16, cc1 = ((lane & 7) ^ (4 + (lane >> 4))) * 16;
      const int nr = n0 + w + (lane & 56);
      unsigned keep;
      asm volatile("s_mov_b32 %0, m0\n\ts_mov_b32 m0, %16\n\ts_nop 0\n\t"
                   "global_load_lds_dwordx4 %1, %9\n\tglobal_load_lds_dwordx4 %1, %10 offset:1024\n\t"
                   "global_load_lds_dwordx4 %2, %11 offset:2048\n\tglobal_load_lds_dwordx4 %2, %12 offset:3072\n\t"
                   "s_add_u32 m0, %16, 0x1000\n\ts_nop 0\n\tglobal_load_lds_dwordx4 %3, %13\n\t"
                   "s_mov_b32 m0, %17\n\ts_nop 0\n\tglobal_load_lds_dwordx4 %4, %14\n\t"
                   "s_add_u32 m0, %17, 0x400\n\ts_nop 0\n\tglobal_load_lds_dwordx4 %5, %14\n\t"
                   "s_add_u32 m0, %17, 0x800\n\ts_nop 0\n\tglobal_load_lds_dwordx4 %6, %14\n\t"
                   "s_add_u32 m0, %17, 0xc00\n\ts_nop 0\n\tglobal_load_lds_dwordx4 %7, %14\n\t"
                   "s_add_u32 m0, %17, 0x1000\n\ts_nop 0\n\tglobal_load_lds_dword %8, %15\n\ts_mov_b32 m0, %0"
                   : "=&s"(keep)
                   : "v"(oh), "v"(oe), "v"(w < 2 ? (lane >> 2) * 128 + (lane & 3) * 16 : 0),
                     "v"(min(nr, N - 1) * 128 + cc0), "v"(min(nr + 64, N - 1) * 128 + cc1), "v"(min(nr + 128, N - 1) * 128 + cc0),
                     "v"(min(n0 + w + 192, N - 1) * 128 + cc1), "v"(mul24(min(n0 + w + min(lane >> 1, 24) * 8, N - 1), 6) + (lane & 1) * 2),
                     "s"(h0), "s"(h0 + (w < 4 ? 4 : 8) * 128 - 1024), "s"(e0p - 2048), "s"(e0p + (w < 4 ? 8 : 4) * 128 - 3072),
                     "s"(we + 129 * 128 + (w & 1) * 64), "s"(hmat), "s"(pos), "s"(wl), "s"(mi)
                   : "memory", "scc");
    }
    asm volatile("s_waitcnt vmcnt(15)" : "+v"(d[0]), "+v"(d[1]), "+v"(d[2]), "+v"(d[3]), "+v"(d[4]), "+v"(d[5]), "+v"(d[6]),
                 "+v"(sv[0]), "+v"(sv[1]), "+v"(sv[2]), "+v"(sv[3]), "+v"(sv[4]), "+v"(sv[5]), "+v"(sv[6]) :: "memory");
    STAMP(10);
    unsigned ent;
    auto sort = [&](auto NS_, auto FULL_) __attribute__((always_inline)) {
      constexpr int NS = decltype(NS_)::value;
      constexpr bool FULL = decltype(FULL_)::value;
      unsigned bk[NS], rank[NS];
      u32x2 rec[NS];
#pragma unroll
      for (int i = 0; i < NS; i++) {
        const int el = i * 512 + tid;
        const unsigned b = mul24(d[i], 85599u) >> 24;
        const unsigned dl = d[i] - mul24(b, NB);
        bk[i] = (FULL || el < ne ? b : NBK + lane) * 4;
        rank[i] = __hip_atomic_fetch_add(&LDS(unsigned, L_CNT + bk[i]), 8u, __ATOMIC_RELAXED, __HIP_MEMORY_SCOPE_WORKGROUP);
        rec[i][0] = e0 + el;
        rec[i][1] = ((mul24(dl >> 3, 72u) + dl + 8) << 16) | sv[i];
      }
      LDS(unsigned, L_BASE + (NBK + lane) * 4) = 0x40000000u;
      barrier_lds();
      const u32x4 cv = LDS(u32x4, L_CNT + lane * 16);
      const unsigned sum = cv[0] + cv[1] + cv[2] + cv[3];
      const unsigned ex = wave_scan_incl(sum) - sum + L_REC;
      const u32x4 bv = {ex, ex + cv[0], ex + cv[0] + cv[1], ex + cv[0] + cv[1] + cv[2]};
      LDS(u32x4, L_BASE + lane * 16) = bv;
      unsigned bs[NS];
#pragma unroll
      for (int i = 0; i < NS; i++) bs[i] = LDS(unsigned, L_BASE + bk[i]);
      ent = ((LDS(unsigned, L_BASE + (tid & 255) * 4) - L_REC) >> 3) | (LDS(unsigned, L_CNT + (tid & 255) * 4) << 9);
      const unsigned lim = L_REC + 8 * SEG + lane * 8;
#pragma unroll
      for (int i = 0; i < NS; i++) LDS(u32x2, FULL ? bs[i] + rank[i] : min(bs[i] + rank[i], lim)) = rec[i];
      barrier_lds();
    };
    if (ne == CH && w != 0) sort(std::integral_constant<int, 6>(), std::true_type());
    else sort(std::integral_constant<int, 7>(), std::false_type());
    LDS(unsigned, L_CNT + tid * 4) = 0;
    STAMP(11);
    {
      const auto rs_out = __builtin_amdgcn_make_buffer_rsrc((void*)(ws + WS_REC + (size_t)bkt * (CH * 8)), 0, ((ne + 1) >> 1) * 16, 0x00020000);
#pragma unroll
      for (int i = 0; i < 4; i++)
        __builtin_amdgcn_raw_buffer_store_b128(LDS(u32x4, L_REC + (i * 512 + tid) * 16), rs_out, (i * 512 + tid) * 16, 0, 16);
    }
    asm volatile("s_waitcnt vmcnt(4)" : "+v"(ep), "+v"(wdv0), "+v"(wdv1), "+v"(xa), "+v"(xb) :: "memory");
    {
      const unsigned tp = wl + (8 * h + ((lane >> 2) & 3)) * 128 + ((lane >> 4) & 1) * 32 + (lane & 3) * 8;
      u32x4 f[2], g[2];
#pragma unroll
      for (int t = 0; t < 2; t++) { f[t] = tr_frag(tp + 64 * t, 512); g[t] = tr_frag(tp + 2048 + 64 * t, 512); }
#pragma unroll
      for (int t = 0; t < 2; t++) {
        if (w < 4) { f[t] = fold(f[t]); g[t] = fold(g[t]); }
        LDS(u32x4, L_WH + (2 * w + t) * 1024 + lane * 16) = f[t];
        LDS(u32x4, wl + 2048 + t * 1024 + lane * 16) = g[t];
      }
      u32x4 x;
      if (w < 2) x = fold(tr_frag(wl + W_STE + (8 * h + ((lane >> 2) & 3)) * 64 + ((lane >> 4) & 1) * 32 + (lane & 3) * 8, 256));
      else { const unsigned m = ((lane & 3) == 0 && ((lane >> 2) & 1) == ((lane >> 4) & 1)) ? ~0u : 0u; x = (u32x4){xa[0] & m, xa[1] & m, xb[0] & m, xb[1] & m}; }
      if (w < 6) LDS(u32x4, L_WJ + w * 1024 + lane * 16) = x;
    }
    const unsigned long long e1 = (((unsigned long long)ep[1] << 32) | ep[0]) + 1;
    tag_lo = __builtin_amdgcn_readfirstlane((unsigned)e1 & 0xffu);
    tag_hi = __builtin_amdgcn_readfirstlane((unsigned)(e1 >> 8));
    const u32x2 g = {ent | (tag_lo << 24), tag_hi};
    asm volatile("s_waitcnt vmcnt(0) lgkmcnt(0)\n\ts_barrier" ::: "memory");
    if (tid < NBK) __builtin_amdgcn_raw_buffer_store_b64(g, rs_tab, tid * (NBK * 8) + bkt * 8, 0, 16);
    STAMP(12);
    {
      u32x2 hb[8];
#pragma unroll
      for (int i = 0; i < 8; i++) hb[i] = LDS(u32x2, mi + r * 128 + ((i ^ ((r >> 1) & 7)) * 16) + h * 8);
#pragma unroll
      for (int t = 0; t < 2; t++) {
        f32x16 P;
#pragma unroll
        for (int i = 0; i < 16; i++) P[i] = 0.f;
#pragma unroll
        for (int s = 0; s < 4; s++) {
          const u32x4 a = {hb[2 * s][0], hb[2 * s][1], hb[2 * s + 1][0], hb[2 * s + 1][1]};
          P = MFMA(as_bf(a), LDS(bf16x8, L_WAVE + (4 + s) * WSZ + 2048 + t * 1024 + lane * 16), P);
        }
        P[15] = bf2f(t ? wdv1 : wdv0);
        P[14] = 0.5f * P[15];
#pragma unroll
        for (int i = 0; i < 16; i++) P[i] *= -LOG2E;
#pragma unroll
        for (int s = 0; s < 2; s++) {
          const u32x4 a = {pk_bf16(P[8 * s], P[8 * s + 1]), pk_bf16(P[8 * s + 2], P[8 * s + 3]),
                           pk_bf16(P[8 * s + 4], P[8 * s + 5]), pk_bf16(P[8 * s + 6], P[8 * s + 7])};
          LDS(u32x4, mi + (2 * s + t) * 1024 + lane * 16) = a;
        }
      }
    }
    if (tid >= 256 && tid < 256 + 26 * 4) {
      const int t = tid - 256, row = t >> 2, sx = (t >> 1) & 1, hh = t & 1;
      const int x = row < DUMMY ? row - 16 * sx - 4 * hh : -1;
      u32x4 v;
      v[0] = x == 0 ? 0x3f80u : x == 1 ? 0x3f800000u : 0u;  v[1] = x == 2 ? 0x3f80u : x == 3 ? 0x3f800000u : 0u;
      v[2] = x == 8 ? 0x3f80u : x == 9 ? 0x3f800000u : 0u;  v[3] = x == 10 ? 0x3f80u : x == 11 ? 0x3f800000u : 0u;
      LDS(u32x4, L_LUT + row * 80 + (t & 3) * 16) = v;
    }
    {
      const u32x4 z4 = {0u, 0u, 0u, 0u};
#pragma unroll
      for (int i = 0; i < 3; i++) { const int o = i * 1024 + lane * 16; if (o < 26 * 80 + 320) LDS(u32x4, wl + W_SI + o) = z4; }
      const u32x2 pg = LDS(u32x2, mi + 4096 + r * 8);
      const float pz = __uint_as_float(pg[1] & 0xffff0000u);
      const f32x4 pv = {bf2f(pg[0] & 0xffffu), pz, __uint_as_float(pg[0] & 0xffff0000u), pz};
      if (lane < DUMMY) LDS(f32x4, wl + W_SI + lane * 80 + 64) = pv;
    }
    STAMP(14);
  }

  f32x16 Y0, Y1;
  f32x4 Yp = {0.f, 0.f, 0.f, 0.f};
#pragma unroll
  for (int i = 0; i < 16; i++) { Y0[i] = 0.f; Y1[i] = 0.f; }
  u32x32 W2; u32x16 PA;
  int jbase = 0, part = -1, grp = w, gother = w;

  auto round = [&](bool first) __attribute__((always_inline)) -> bool {
    const int lane = lane_id(), tid = w * 64 + lane, r = lane & 31, h = lane >> 5;
    const unsigned zr = opaque(0);
    if (!first) { LDS(unsigned, L_CNT + tid * 4) = zr; barrier_lds(); }
    LDS(unsigned, L_BASE + lane * 4) = 0x40000000u;
    unsigned tb[8];
    {
      u32x2 t;
      for (int spin = 0;; spin++) {
        asm volatile("buffer_load_dwordx2 %0, %1, %2, %3 offen sc1\n\ts_waitcnt vmcnt(0)"
                     : "=&v"(t) : "v"(w * 256 + r * 8), "s"(rs_tab), "s"(bkt * (NBK * 8)) : "memory");
        if (__builtin_amdgcn_ballot_w64(t[1] != tag_hi || (t[0] >> 24) != tag_lo) == 0) break;
        if (spin > (1 << 20)) { t[0] = 0; break; }
        __builtin_amdgcn_s_sleep(POLL_SLEEP);
      }
      LDS(unsigned, L_WAVE + w * WSZ + W_STE + r * 4) = t[0];
      const u32x4 t0 = LDS(u32x4, L_WAVE + w * WSZ + W_STE + (lane >> 4) * 32), t1 = LDS(u32x4, L_WAVE + w * WSZ + W_STE + (lane >> 4) * 32 + 16);
#pragma unroll
      for (int p = 0; p < 4; p++) { tb[p] = t0[p]; tb[4 + p] = t1[p]; }
    }
    STAMP(8);
    u32x2 rc[10];
    unsigned lmax = 0, nov = 0;
    bool big;
    {
      const unsigned jj = lane & (LG - 1), j = jbase + jj, o0 = mul24((unsigned)tid / LG, 8 * CH * 8) + j * 8, oob = 0x80000000u + zr;
      unsigned off[8], len[8];
#pragma unroll
      for (int p = 0; p < 8; p++) {
        len[p] = (tb[p] >> 12) & 0xfffu;
        if (part >= 0 && part != 2 * p + (w & 1)) len[p] = 0;
        off[p] = o0 + (tb[p] & 0xfffu) * 8;
        const unsigned a = j < len[p] ? off[p] : oob;
        asm volatile("buffer_load_dwordx2 %0, %1, %2, %3 offen sc1" : "=v"(rc[p]) : "v"(a), "s"(rs_rec), "s"(p * CH * 8) : "memory");
      }
      asm volatile("" : "+v"(len[0]), "+v"(len[1]), "+v"(len[2]), "+v"(len[3]), "+v"(len[4]), "+v"(len[5]), "+v"(len[6]), "+v"(len[7]));
      unsigned a9 = oob;
#pragma unroll
      for (int p = 0; p < 8; p++) {
        const unsigned ov = (unsigned)min(max((int)len[p] - (jbase + LG), 0), LG);
        a9 = jj - nov < ov ? off[p] - nov * 8 + (p * CH * 8 + LG * 8) : a9;
        nov += ov;
      }
      asm volatile("buffer_load_dwordx2 %0, %1, %2, 0 offen sc1" : "=v"(rc[8]) : "v"(a9), "s"(rs_rec) : "memory");
#pragma unroll
      for (int p = 0; p < 8; p++) lmax = max(lmax, tb[p] & 0xfff000u);
      rc[9] = (u32x2){zr, zr};
      big = __builtin_amdgcn_ballot_w64(nov > LG) != 0;
      if (big) {
        unsigned a10 = oob, s1 = 0;
#pragma unroll
        for (int p = 0; p < 8; p++) {
          const unsigned ov = (unsigned)min(max((int)len[p] - (jbase + LG), 0), LG);
          a10 = jj + LG - s1 < ov ? off[p] - s1 * 8 + (p * CH * 8 + LG * 16) : a10;
          s1 += ov;
        }
        asm volatile("buffer_load_dwordx2 %0, %1, %2, 0 offen sc1" : "+v"(rc[9]) : "v"(a10), "s"(rs_rec) : "memory");
      }
    }
    unsigned sink = zr;
    if (HPF) {
      const unsigned a = lane < HPF ? (((blockIdx.x >> 3) * 8 + w) * HPF + lane) * 128 : 0x80000000u + zr;
      asm volatile("buffer_load_dword %0, %1, %2, 0 offen" : "+{v3}"(sink) : "v"(a), "s"(rs_h) : "memory");
    }
    STAMP(7);
    if (first) {
#pragma unroll
      for (int i = 0; i < 8; i++) {
        const u32x4 v = LDS(u32x4, L_WAVE + (i >> 1) * WSZ + 2048 + (i & 1) * 1024 + lane * 16);
#pragma unroll
        for (int j = 0; j < 4; j++) W2[4 * i + j] = v[j];
      }
    }
    STAMP(13);
    if ((lmax >> 12) > (unsigned)jbase + 2 * LG || nov > 2 * LG) __hip_atomic_fetch_or(&LDS(unsigned, L_FLAG), nov > 2 * LG ? 3u : 1u, __ATOMIC_RELAXED, __HIP_MEMORY_SCOPE_WORKGROUP);
    unsigned mk1 = (lane & 7) >= 1 ? ~zr : zr, mk2 = (lane & 7) >= 2 ? ~zr : zr, sg0 = L_REC + (lane >> 3) * SEG;
    unsigned lim = L_REC + 8 * SEG + lane * 8, own = n0 + min(8 * (r & 3) + (r >> 2), 24) * 8;
    unsigned ce = ((lane & 1) ^ ((lane >> 4) & 1)) * 16, cp = (lane & 1) * 2;
    unsigned cc0 = ((lane & 7) ^ (lane >> 4)) * 16, cc1 = ((lane & 7) ^ (4 + (lane >> 4))) * 16;
    unsigned q_s4 = L_REC + (lane >> 3) * 32 + 4;
    unsigned q_r2 = L_REC + (4 * ((lane >> 1) & 7) + (lane >> 4)) * 8;
    unsigned q_r1 = L_REC + (4 * (r & 7) + (r >> 3)) * 8 + 4;
    asm volatile("" : "+v"(mk1), "+v"(mk2), "+v"(sg0), "+v"(lim), "+v"(own), "+v"(ce), "+v"(cp), "+v"(cc0), "+v"(cc1), "+v"(q_s4),
                 "+v"(q_r2), "+v"(q_r1));
    asm volatile("s_waitcnt vmcnt(%10)" : "+v"(rc[0]), "+v"(rc[1]), "+v"(rc[2]), "+v"(rc[3]), "+v"(rc[4]), "+v"(rc[5]), "+v"(rc[6]),
                 "+v"(rc[7]), "+v"(rc[8]), "+v"(rc[9]) : "i"(HPF ? 1 : 0) : "memory");
    STAMP(1);
    unsigned rk[10], ky[10];
#pragma unroll
    for (int p = 0; p < 10; p++) {
      if (p == 9 && !big) { ky[p] = 0; rk[p] = 0; break; }
      ky[p] = max((rc[p][1] >> 9) & 0x7fcu, (unsigned)lane * 4);
      rk[p] = __hip_atomic_fetch_add(&LDS(unsigned, L_CNT + ky[p]), 8u + zr, __ATOMIC_RELAXED, __HIP_MEMORY_SCOPE_WORKGROUP);
    }
    if (EPF) {
#pragma unroll
      for (int p = 0; p < 9; p++) {
        const unsigned a = (ky[p] & 0x47cu) - 0x400u < (unsigned)EPF * 4 ? rc[p][0] << 5 : 0x80000000u + zr;
        asm volatile("buffer_load_dword %0, %1, %2, 0 offen" : "+{v3}"(sink) : "v"(a), "s"(rs_ea) : "memory");
      }
    }
    barrier_lds();
    STAMP(2);
    unsigned g;
    const unsigned flagv = LDS(unsigned, L_FLAG);
    {
      const u32x4 cv = LDS(u32x4, L_CNT + 1024 + lane * 16);
      const unsigned sum = cv[0] + cv[1] + cv[2] + cv[3];
      g = sum + (__builtin_amdgcn_update_dpp(0, (int)sum, 0x111, 0xf, 0xf, true) & mk1);
      g += __builtin_amdgcn_update_dpp(0, (int)g, 0x112, 0xf, 0xf, true) & mk2;
      g += __builtin_amdgcn_update_dpp(0, (int)g, 0x114, 0xf, 0xa, false);
      const unsigned ex = g - sum + sg0;
      const u32x4 bv = {ex, ex + cv[0], ex + cv[0] + cv[1], ex + cv[0] + cv[1] + cv[2]};
      LDS(u32x4, L_BASE + 1024 + lane * 16) = bv;
    }
    unsigned bs[9];
#pragma unroll
    for (int p = 0; p < 9; p++) bs[p] = LDS(unsigned, L_BASE + ky[p]);
    const unsigned flag = __builtin_amdgcn_readfirstlane(flagv);
    const bool again = flag & 1;
    const bool over = (flag & 2) || __builtin_amdgcn_ballot_w64(g > (unsigned)(SEG - 256)) != 0;
    if (!over) {
#pragma unroll
      for (int p = 0; p < 9; p++) LDS(u32x2, min(bs[p] + rk[p], lim)) = rc[p];
      if (big) LDS(u32x2, min(LDS(unsigned, L_BASE + ky[9]) + rk[9], lim)) = rc[9];
    }
    if (first) {
      const unsigned key = (g << 3) | (lane >> 3);
      unsigned ks[8], rank = 0;
#pragma unroll
      for (int k = 0; k < 8; k++) ks[k] = __builtin_amdgcn_readlane(key, 8 * k + 7);
#pragma unroll
      for (int k = 0; k < 8; k++) rank += ks[k] > key;
      const unsigned long long sel = 0x8080808080808080ull;
      grp = __builtin_ctzll(__builtin_amdgcn_ballot_w64(rank == (unsigned)(w < 4 ? w : 11 - w)) & sel) >> 3;
      gother = __builtin_ctzll(__builtin_amdgcn_ballot_w64(rank == (unsigned)(w < 4 ? 7 - w : w - 4)) & sel) >> 3;
      wl = L_WAVE + grp * WSZ; mi = L_MI + grp * MISZ;
    }
    unsigned segn = over ? 0u : (unsigned)__builtin_amdgcn_readlane(g, 8 * grp + 7);
    const unsigned seg0 = grp * SEG;
    const int nt = (segn + 255) >> 8;
    {
      const unsigned dum = (DUMMY * 80u + 8 + grp) << 16;
      const u32x2 pad = {zr, dum + zr}, row = {zr, min(own + grp, (unsigned)N - 1) | dum};
      LDS(u32x2, L_REC + seg0 + segn + r * 8) = pad;
      LDS(u32x2, L_REC + seg0 + nt * 256 + r * 8) = row;
    }
    barrier_lds();
    STAMP(3);

    const unsigned o_s4 = q_s4 + seg0, o_r2 = q_r2 + seg0, o_r1 = q_r1 + seg0;
    {
      unsigned ns[4];
#pragma unroll
      for (int i = 0; i < 4; i++) ns[i] = LDS(uint16_t, o_s4 + i * 8);
      const u32x2 nr2 = LDS(u32x2, o_r2);
      dma_tile(hmat, ea, pos, ns[0] * 128 + cc0, ns[1] * 128 + cc1, ns[2] * 128 + cc0, ns[3] * 128 + cc1,
               (nr2[0] << 5) | ce, (nr2[1] & 0xffffu) * 6 + cp, wl);
    }
    if (first) {
#pragma unroll
      for (int i = 0; i < 4; i++) {
        const u32x4 v = LDS(u32x4, mi + i * 1024 + lane * 16);
#pragma unroll
        for (int j = 0; j < 4; j++) PA[4 * i + j] = v[j];
      }
      if (bkt == 0 && tid == 0) {
        const u32x2 e1 = {tag_lo | (tag_hi << 8), tag_hi >> 24};
        asm volatile("global_store_dwordx2 %0, %1, %2 sc1" :: "v"(zr), "v"(e1), "s"(ws + WS_EPOCH) : "memory");
      }
    }
    const unsigned nother = (__builtin_amdgcn_readlane(g, 8 * gother + 7) + 255) >> 8;
    const bool last_round = !over && !again && (part < 0 || part == 15);
    STAMP(4);

    {
      u32x16 A0, B0; u32x8 A1;
      const unsigned bias = 8 + grp;
#pragma unroll
      for (int s = 0; s < 4; s++) A0[s] = wl + W_STH + r * 128 + (((2 * s + h) ^ ((r >> 1) & 7)) * 16);
      A0[4] = wl + r * 32 + ((h ^ ((r >> 3) & 1)) * 16);
      A0[5] = wl + r * 8;
      A0[6] = wl + r * 8 + h * 4;
      A0[7] = L_LUT + h * 16 - bias;
      A0[8] = wl + r * 2 - bias;
      A0[9] = wl + min(r, DUMMY) * 80 + h * 16;
      A0[10] = mi + r * MIL + h * 32;
      A0[11] = mi + (8 * h + ((lane >> 2) & 3)) * MIL + (4 * ((lane >> 4) & 1) + (lane & 3)) * 8;
      A0[12] = wl + h * 128 + r * 2;
      {
        const int c = lane & 3, ok = c < 3 && (lane & 8) == 0 && ((lane >> 2) & 1) == ((lane >> 4) & 1);
        A0[13] = wl + (ok ? (c == 0 ? 0 : c == 1 ? 128 : 64) : 256) + h * 16;
      }
      A0[14] = L_WJ + lane * 16;
      A0[15] = wl + W_SI + 64 + h * 8 - bias;
#pragma unroll
      for (int i = 0; i < 16; i++) B0[i] = zr;
      B0[1] = cc0 + 3072; B0[3] = cc1 + 2048; B0[5] = cc0 + 1024; B0[7] = cc1; B0[9] = ce; B0[11] = cp;
      A1[0] = o_s4 + 256; A1[1] = o_r2 + 256; A1[2] = o_r1 + 2; A1[3] = zr;
      A1[4] = (h ? 0u : 0x3f80u) + zr; A1[5] = zr; A1[6] = 0x3f800000u + zr; A1[7] = zr;
      const auto rsh = __builtin_amdgcn_make_buffer_rsrc((void*)(hmat - 3072), 128, N, 0x00020000);
      const auto rse = __builtin_amdgcn_make_buffer_rsrc((void*)ea, 32, E, 0x00020000);
      const auto rsp = __builtin_amdgcn_make_buffer_rsrc((void*)(pos - 1024), 6, N, 0x00020000);
      unsigned cnt = nt, keep;
#define PRIO_A 14
#define PRIO_B 7
      const int lastv = __builtin_amdgcn_readfirstlane(opaque(last_round ? 1 : 0));
      const int thr = __builtin_amdgcn_readfirstlane(w >= 4 ? max((int)(nother * PRIO_A - cnt * PRIO_B) >> 5, 0) : -1);
      asm volatile(LOOP_ASM
                   : "+{v[52:67]}"(Y0), "+{v[68:83]}"(Y1), "+{v[84:87]}"(Yp), "+{v[216:231]}"(B0), "+{v[232:247]}"(A0), "+{v[248:255]}"(A1),
                     "+{v3}"(sink), [nt] "+s"(cnt), [keep] "=&s"(keep)
                   : "{v[4:35]}"(W2), "{v[36:51]}"(PA), [rsh] "s"(rsh), [rse] "s"(rse), [rsp] "s"(rsp),
                     [b1] "s"(wl), [thr] "s"(thr), [last] "s"(lastv)
                   : "memory", "scc", "vcc", CLOBBER_V);
    }
    if (over) part = 0;
    else if (part >= 0 && part < 15) part++;
    else { part = -1; jbase += 2 * LG; }
    return last_round;
  };
  if (!round(true)) {
    do {
      wait_vm0();
      barrier_lds();
      jbase = __builtin_amdgcn_readfirstlane(jbase); part = __builtin_amdgcn_readfirstlane(part);
    } while (!round(false));
  }
  STAMP(5);

  {
    const int lane = lane_id(), r = lane & 31, h = lane >> 5;
    Yp[0] *= -LN2; Yp[1] *= -LN2; Yp[2] *= -LN2;
    const auto rs_o = __builtin_amdgcn_make_buffer_rsrc((void*)out, 0, N * 128, 0x00020000);
#pragma unroll
    for (int i = 0; i < 4; i++) {
      const int q = 8 * i + (lane >> 3), dl = q * 8 + grp;
      const u32x4 v = LDS(u32x4, mi + q * MIP + (lane & 7) * 16);
      if (dl < NB) __builtin_amdgcn_raw_buffer_store_b128(v, rs_o, (n0 + dl) * 128 + (lane & 7) * 16, 0, OUT_AUX);
    }
    const int dl = r * 8 + grp;
    if (h == 0 && dl < NB && n0 + dl < N) {
      const f32x4 pd = LDS(f32x4, wl + W_SI + r * 80 + 64);
      uint16_t* po = out + (size_t)N * 64 + (size_t)(n0 + dl) * 3;
      const unsigned xy = pk_bf16(pd[0] + Yp[0], pd[2] + Yp[1]), zz = pk_bf16(pd[1] + Yp[2], 0.f);
      po[0] = (uint16_t)xy; po[1] = (uint16_t)(xy >> 16); po[2] = (uint16_t)zz;
    }
  }
  STAMP(6);
}

}

#define STAMP_PASS1
#define STAMP_PASS2

extern "C" void kernel_launch(void* const* d_in, const int* in_sizes, int n_in, void* d_out, int out_size,
                              void* d_ws, size_t ws_size, hipStream_t stream) {
  const int N = in_sizes[0] / 64, E = in_sizes[3];
  if (N > NBK * NB || E > NBK * CH || N < 1 || E < 1) return;
  (void)hipFuncSetAttribute((const void*)egnn_kernel, hipFuncAttributeMaxDynamicSharedMemorySize, L_TOTAL);
  egnn_kernel<<<NBK, 512, L_TOTAL, stream>>>((const char*)d_in[0], (const char*)d_in[1], (const char*)d_in[2], (const int*)d_in[3],
                                             (const int*)d_in[4], (const char*)d_in[5], (const char*)d_in[6], (const char*)d_in[7],
                                             (char*)d_ws, (uint16_t*)d_out, N, E STAMP_PASS2);
}
```

```cpp
#include <hip/hip_runtime.h>
#include <stdint.h>
#include <type_traits>

namespace {

typedef __bf16 bf16x8 __attribute__((ext_vector_type(8)));
typedef __bf16 bf16x2 __attribute__((ext_vector_type(2)));
typedef float f32x16 __attribute__((ext_vector_type(16)));
typedef float f32x4 __attribute__((ext_vector_type(4)));
typedef unsigned u32x4 __attribute__((ext_vector_type(4)));
typedef unsigned u32x2 __attribute__((ext_vector_type(2)));
typedef unsigned u32x8 __attribute__((ext_vector_type(8)));
typedef unsigned u32x16 __attribute__((ext_vector_type(16)));
typedef unsigned u32x32 __attribute__((ext_vector_type(32)));
typedef short s16x4 __attribute__((ext_vector_type(4)));
typedef __attribute__((address_space(3))) s16x4* lds_s16x4_p;

#define DI __device__ __forceinline__
#define MFMA(a, b, c) __builtin_amdgcn_mfma_f32_32x32x16_bf16((a), (b), (c), 0, 0, 0)

#define POLL_SLEEP 2
#define HPF 0
#define EPF 2
#define OUT_AUX 16
constexpr int NBK = 256;
constexpr int LG = 16;
constexpr int NB = 196;
constexpr int CH = 3136;
constexpr float LOG2E = 1.4426950408889634f;
constexpr float LN2 = 0.6931471805599453f;

constexpr size_t WS_EPOCH = 0;
constexpr size_t WS_TAB = 256;
constexpr size_t WS_REC = WS_TAB + (size_t)NBK * NBK * 8;

constexpr int W_STH = 0, W_STE = 4096, W_STP = 5120, W_SI = 5376, W_TI = W_SI + 26 * 80, WSZ = W_TI + 320;
constexpr int DUMMY = 25;
constexpr int MIP = 144, MISZ = 32 * MIP;
constexpr int MIL = 80;
constexpr int SEG = 17 * 256;
constexpr int L_CNT = 0;
constexpr int L_BASE = L_CNT + 2048;
constexpr int L_FLAG = L_CNT + 512;
constexpr int L_REC = L_BASE + 2048;
constexpr int L_LUT = L_REC + 8 * SEG + 512;
constexpr int L_WAVE = L_LUT + 26 * 80;
constexpr int L_MI = L_WAVE + 8 * WSZ;
constexpr int L_WJ = L_MI + 8 * MISZ;
constexpr int L_WH = L_WJ + 6 * 1024;
constexpr int L_TOTAL = L_WH + 16 * 1024;
static_assert(L_TOTAL <= 163840, "LDS");
static_assert(WSZ % 16 == 0 && L_MI % 16 == 0 && L_LUT % 16 == 0, "align");

#pragma clang diagnostic ignored "-Wint-to-pointer-cast"
#define LDS(T, off) (*(__attribute__((address_space(3))) T*)(unsigned)(off))
DI unsigned mul24(unsigned a, unsigned b) { return (unsigned)__umul24(a, b); }
DI float bf2f(unsigned lo16) { return __uint_as_float(lo16 << 16); }
DI unsigned pk_bf16(float a, float b) { bf16x2 v = {(__bf16)a, (__bf16)b}; return __builtin_bit_cast(unsigned, v); }
DI bf16x8 as_bf(u32x4 v) { return __builtin_bit_cast(bf16x8, v); }
DI int opaque(int x) { asm volatile("" : "+v"(x)); return x; }
DI int lane_id() {
  int x; asm volatile("v_mbcnt_lo_u32_b32 %0, -1, 0\n\tv_mbcnt_hi_u32_b32 %0, -1, %0" : "=v"(x)); return x;
}
DI void wait_vm0() { asm volatile("s_waitcnt vmcnt(0)" ::: "memory"); }
DI void wait_lgkm0() { asm volatile("s_waitcnt lgkmcnt(0)" ::: "memory"); }
DI void barrier_lds() { asm volatile("s_waitcnt lgkmcnt(0)\n\ts_barrier" ::: "memory"); }
DI void dma16(const void* g, unsigned off, unsigned l) {
  unsigned keep;
  asm volatile("s_mov_b32 %0, m0\n\ts_mov_b32 m0, %3\n\ts_nop 0\n\tglobal_load_lds_dwordx4 %1, %2\n\ts_mov_b32 m0, %0"
               : "=&s"(keep) : "v"(off), "s"(g), "s"(l) : "memory");
}
DI void dma4(const void* g, unsigned off, unsigned l) {
  unsigned keep;
  asm volatile("s_mov_b32 %0, m0\n\ts_mov_b32 m0, %3\n\ts_nop 0\n\tglobal_load_lds_dword %1, %2\n\ts_mov_b32 m0, %0"
               : "=&s"(keep) : "v"(off), "s"(g), "s"(l) : "memory");
}
DI void dma_tile(const void* hm, const void* ea, const void* ps, unsigned o0, unsigned o1, unsigned o2, unsigned o3,
                 unsigned oe, unsigned op, unsigned l) {
  unsigned keep;
  asm volatile("s_mov_b32 %0, m0\n\t"
               "s_add_u32 m0, %10, 0x1000\n\ts_nop 0\n\tglobal_load_lds_dwordx4 %5, %8\n\t"
               "s_mov_b32 m0, %10\n\ts_nop 0\n\tglobal_load_lds_dwordx4 %1, %7\n\t"
               "s_add_u32 m0, %10, 0x400\n\ts_nop 0\n\tglobal_load_lds_dwordx4 %2, %7\n\t"
               "s_add_u32 m0, %10, 0x800\n\ts_nop 0\n\tglobal_load_lds_dwordx4 %3, %7\n\t"
               "s_add_u32 m0, %10, 0xc00\n\ts_nop 0\n\tglobal_load_lds_dwordx4 %4, %7\n\t"
               "s_add_u32 m0, %10, 0x1400\n\ts_nop 0\n\tglobal_load_lds_dword %6, %9\n\t"
               "s_mov_b32 m0, %0"
               : "=&s"(keep) : "v"(o0), "v"(o1), "v"(o2), "v"(o3), "v"(oe), "v"(op), "s"(hm), "s"(ea), "s"(ps), "s"(l)
               : "memory", "scc");
}
template <int CTRL, int ROWMASK> DI unsigned dpp_add(unsigned x) {
  return x + (unsigned)__builtin_amdgcn_update_dpp(0, (int)x, CTRL, ROWMASK, 0xf, false);
}
DI unsigned wave_scan_incl(unsigned x) {
  x = dpp_add<0x111, 0xf>(x); x = dpp_add<0x112, 0xf>(x); x = dpp_add<0x114, 0xf>(x); x = dpp_add<0x118, 0xf>(x);
  x = dpp_add<0x142, 0xa>(x); x = dpp_add<0x143, 0xc>(x);
  return x;
}
#define STAMP(i)
#define STAMP_ARG

#define CLOBBER_V "v88", "v89", "v90", "v91", "v92", "v93", "v94", "v95", "v96", "v97", "v98", "v99", "v100", "v101", "v102", "v103", "v104", "v105", "v106", "v107", "v108", "v109", "v110", "v111", "v112", "v113", "v114", "v115", "v116", "v117", "v118", "v119", "v120", "v121", "v122", "v123", "v124", "v125", "v126", "v127", "v128", "v129", "v130", "v131", "v132", "v133", "v134", "v135", "v136", "v137", "v138", "v139", "v140", "v141", "v142", "v143", "v144", "v145", "v146", "v147", "v148", "v149", "v150", "v151", "v152", "v153", "v154", "v155", "v156", "v157", "v158", "v159", "v160", "v161", "v162", "v163", "v164", "v165", "v166", "v167", "v168", "v169", "v170", "v171", "v172", "v173", "v174", "v175", "v176", "v177", "v178", "v179", "v180", "v181", "v182", "v183", "v184", "v185", "v186", "v187", "v188", "v189", "v190", "v191", "v192", "v193", "v194", "v195", "v196", "v197", "v198", "v199", "v200", "v201", "v202", "v203", "v204", "v205", "v206", "v207", "v208", "v209", "v210", "v211", "v212", "v213", "v214", "v215"
#define LOOP_ASM \
  "s_mov_b32 %[keep], m0\n\t" \
  "ds_read_u16 v216, v248 offset:0\n\t" \
  "ds_read_u16 v218, v248 offset:8\n\t" \
  "ds_read_u16 v220, v248 offset:16\n\t" \
  "ds_read_u16 v222, v248 offset:24\n\t" \
  "ds_read_b32 v224, v249\n\t" \
  "ds_read_u16 v226, v249 offset:4\n\t" \
  "ds_read_u16 v228, v250\n\t" \
  "ds_read_b128 v[96:99], v246 offset:0\n\t" \
  "ds_read_b128 v[92:95], v246 offset:1024\n\t" \
  "v_mov_b32 v192, 0\n\t" \
  "v_mov_b32 v193, 0\n\t" \
  "v_mov_b32 v194, 0\n\t" \
  "v_mov_b32 v195, 0\n\t" \
  "v_mov_b32 v196, 0\n\t" \
  "v_mov_b32 v197, 0\n\t" \
  "v_mov_b32 v198, 0\n\t" \
  "v_mov_b32 v199, 0\n\t" \
  "v_mov_b32 v214, 0\n\t" \
  "v_mov_b32 v215, 0\n\t" \
  "v_mov_b32 v88, 0\n\t" \
  "v_add_u32 v248, 0x100, v248\n\t" \
  "v_add_u32 v249, 0x100, v249\n\t" \
  "v_add_u32 v250, 0x100, v250\n\t" \
  "s_waitcnt lgkmcnt(0)\n\t" \
  "v_add_u32 v230, v247, v228\n\t" \
  "v_add_u32 v231, v239, v228\n\t" \
  "ds_read_b64 v[208:209], v230\n\t" \
  "ds_read_b128 v[188:191], v231 offset:32\n\t" \
  "ds_read_b128 v[184:187], v231\n\t" \
  "v_add_u32 v229, v240, v228\n\t" \
  "ds_write_b16_d16_hi v229, v254 offset:5376\n\t" \
  "s_waitcnt vmcnt(0)\n\t" \
  "ds_read_b128 v[164:167], v232\n\t" \
  "ds_read_b128 v[168:171], v233\n\t" \
  "ds_read_b128 v[172:175], v234\n\t" \
  "ds_read_b128 v[176:179], v235\n\t" \
  "ds_read_b128 v[180:183], v236 offset:4096\n\t" \
  "ds_read_u16_d16_hi v210, v238 offset:5120\n\t" \
  "ds_read_u16_d16_hi v211, v237 offset:5126\n\t" \
  "s_waitcnt lgkmcnt(0)\n\t" \
  "s_cmp_lt_i32 %[nt], 1\n\t" \
  "s_cbranch_scc1 7f\n\t" \
  "s_add_u32 m0, %[b1], 4096\n\t" \
  "s_nop 0\n\t" \
  "buffer_load_dwordx4 v[224:225], %[rse], 0 idxen offen lds\n\t" \
  "buffer_load_dword v[226:227], %[rsp], 0 idxen offen offset:1024 lds\n\t" \
  "s_mov_b32 m0, %[b1]\n\t" \
  "s_nop 0\n\t" \
  "buffer_load_dwordx4 v[216:217], %[rsh], 0 idxen offen  lds\n\t" \
  "buffer_load_dwordx4 v[218:219], %[rsh], 0 idxen offen offset:1024 lds\n\t" \
  "buffer_load_dwordx4 v[220:221], %[rsh], 0 idxen offen offset:2048 lds\n\t" \
  "buffer_load_dwordx4 v[222:223], %[rsh], 0 idxen offen offset:3072 lds\n\t" \
  "7:\n\t" \
  "ds_read_u16 v216, v248 offset:0\n\t" \
  "ds_read_u16 v218, v248 offset:8\n\t" \
  "ds_read_u16 v220, v248 offset:16\n\t" \
  "ds_read_u16 v222, v248 offset:24\n\t" \
  "ds_read_b32 v224, v249\n\t" \
  "ds_read_u16 v226, v249 offset:4\n\t" \
  "ds_read_u16 v228, v250\n\t" \
  "v_add_u32 v250, 0x100, v250\n\t" \
  "v_sub_f32 v212, v208, v210\n\t" \
  "v_sub_f32 v213, v209, v211\n\t" \
  "v_mul_f32 v208, v212, v212\n\t" \
  "v_mul_f32 v209, v213, v213\n\t" \
  "v_cvt_pk_bf16_f32 v191, v209, v208\n\t" \
  "v_mfma_f32_32x32x16_bf16 v[100:115], v[36:39], v[184:187], 0\n\t" \
  "v_mfma_f32_32x32x16_bf16 v[100:115], v[4:7], v[164:167], v[100:115]\n\t" \
  "v_mfma_f32_32x32x16_bf16 v[100:115], v[12:15], v[168:171], v[100:115]\n\t" \
  "v_mfma_f32_32x32x16_bf16 v[100:115], v[20:23], v[172:175], v[100:115]\n\t" \
  "v_mfma_f32_32x32x16_bf16 v[100:115], v[28:31], v[176:179], v[100:115]\n\t" \
  "v_mfma_f32_32x32x16_bf16 v[100:115], v[96:99], v[180:183], v[100:115]\n\t" \
  "v_mfma_f32_32x32x16_bf16 v[100:115], v[44:47], v[188:191], v[100:115]\n\t" \
  "s_cmp_eq_u32 %[nt], 0\n\t" \
  "s_cbranch_scc1 6f\n\t" \
  "s_nop 10\n\t" \
  "v_exp_f32 v132, v100\n\t" \
  "v_exp_f32 v133, v101\n\t" \
  "v_exp_f32 v134, v102\n\t" \
  "v_exp_f32 v135, v103\n\t" \
  "v_exp_f32 v136, v104\n\t" \
  "v_exp_f32 v137, v105\n\t" \
  "v_exp_f32 v138, v106\n\t" \
  "v_exp_f32 v139, v107\n\t" \
  "v_exp_f32 v140, v108\n\t" \
  "v_exp_f32 v141, v109\n\t" \
  "v_exp_f32 v142, v110\n\t" \
  "v_exp_f32 v143, v111\n\t" \
  "v_exp_f32 v144, v112\n\t" \
  "v_exp_f32 v145, v113\n\t" \
  "v_exp_f32 v146, v114\n\t" \
  "v_exp_f32 v147, v115\n\t" \
  "s_nop 1\n\t" \
  "v_mfma_f32_32x32x8bf16_1k v[132:147], v[252:253], v[252:253], v[132:147]\n\t" \
  "v_mfma_f32_32x32x16_bf16 v[116:131], v[40:43], v[184:187], 0\n\t" \
  "v_mfma_f32_32x32x16_bf16 v[116:131], v[48:51], v[188:191], v[116:131]\n\t" \
  "s_cmp_lt_i32 %[thr], 0\n\t" \
  "s_cbranch_scc1 2f\n\t" \
  "s_setprio 1\n\t" \
  "s_branch 2f\n\t" \
  "1:\n\t" \
  "ds_write_b128 v242, v[152:155] offset:16\n\t" \
  "ds_read_b64_tr_b16 v[200:201], v243 offset:0\n\t" \
  "ds_read_b64_tr_b16 v[202:203], v243 offset:320\n\t" \
  "ds_read_b64_tr_b16 v[204:205], v243 offset:1280\n\t" \
  "ds_read_b64_tr_b16 v[206:207], v243 offset:1600\n\t" \
  "ds_read_b128 v[96:99], v246 offset:5120\n\t" \
  "v_exp_f32 v132, v100\n\t" \
  "v_exp_f32 v133, v101\n\t" \
  "v_exp_f32 v134, v102\n\t" \
  "v_exp_f32 v135, v103\n\t" \
  "s_waitcnt lgkmcnt(6)\n\t" \
  "v_mfma_f32_16x16x32_bf16 v[88:91], v[92:95], v[148:151], v[88:91]\n\t" \
  "ds_read_b128 v[92:95], v246 offset:1024\n\t" \
  "v_exp_f32 v136, v104\n\t" \
  "v_exp_f32 v137, v105\n\t" \
  "v_exp_f32 v138, v106\n\t" \
  "v_exp_f32 v139, v107\n\t" \
  "s_waitcnt lgkmcnt(4)\n\t" \
  "v_mfma_f32_32x32x16_bf16 v[68:83], v[200:203], v[192:195], v[68:83]\n\t" \
  "ds_read_u16 v216, v248 offset:0\n\t" \
  "ds_read_u16 v218, v248 offset:8\n\t" \
  "ds_read_u16 v220, v248 offset:16\n\t" \
  "ds_read_u16 v222, v248 offset:24\n\t" \
  "ds_read_b32 v224, v249\n\t" \
  "ds_read_u16 v226, v249 offset:4\n\t" \
  "v_exp_f32 v140, v108\n\t" \
  "v_exp_f32 v141, v109\n\t" \
  "v_exp_f32 v142, v110\n\t" \
  "v_exp_f32 v143, v111\n\t" \
  "s_waitcnt lgkmcnt(7)\n\t" \
  "v_mfma_f32_16x16x32_bf16 v[88:91], v[96:99], v[152:155], v[88:91]\n\t" \
  "v_exp_f32 v144, v112\n\t" \
  "v_exp_f32 v145, v113\n\t" \
  "v_exp_f32 v146, v114\n\t" \
  "v_exp_f32 v147, v115\n\t" \
  "v_mfma_f32_32x32x16_bf16 v[68:83], v[204:207], v[196:199], v[68:83]\n\t" \
  "v_mfma_f32_32x32x16_bf16 v[116:131], v[40:43], v[184:187], 0\n\t" \
  "v_mfma_f32_32x32x8bf16_1k v[132:147], v[252:253], v[252:253], v[132:147]\n\t" \
  "v_mfma_f32_32x32x16_bf16 v[116:131], v[48:51], v[188:191], v[116:131]\n\t" \
  "2:\n\t" \
  "v_mul_f32 v214, v214, v88\n\t" \
  "v_mul_f32 v215, v215, v88\n\t" \
  "v_cvt_pk_bf16_f32 v214, v214, v215\n\t" \
  "ds_write_b16 v244, v214 offset:7456\n\t" \
  "ds_write_b16_d16_hi v244, v214 offset:7520\n\t" \
  "ds_read_b128 v[200:203], v245 offset:7456\n\t" \
  "ds_read_b128 v[204:207], v245 offset:7488\n\t" \
  "v_mov_b32 v214, v212\n\t" \
  "v_mov_b32 v215, v213\n\t" \
  "s_waitcnt lgkmcnt(10)\n\t" \
  "v_mfma_f32_32x32x16_bf16 v[116:131], v[92:95], v[180:183], v[116:131]\n\t" \
  "v_rcp_f32 v132, v132\n\t" \
  "v_rcp_f32 v133, v133\n\t" \
  "v_rcp_f32 v134, v134\n\t" \
  "v_rcp_f32 v135, v135\n\t" \
  "v_mfma_f32_32x32x16_bf16 v[116:131], v[8:11], v[164:167], v[116:131]\n\t" \
  "v_rcp_f32 v136, v136\n\t" \
  "v_rcp_f32 v137, v137\n\t" \
  "v_rcp_f32 v138, v138\n\t" \
  "v_rcp_f32 v139, v139\n\t" \
  "v_mfma_f32_32x32x16_bf16 v[116:131], v[16:19], v[168:171], v[116:131]\n\t" \
  "v_mul_f32 v132, v100, v132\n\t" \
  "v_mul_f32 v133, v101, v133\n\t" \
  "v_mul_f32 v134, v102, v134\n\t" \
  "v_mul_f32 v135, v103, v135\n\t" \
  "v_cvt_pk_bf16_f32 v132, v132, v133\n\t" \
  "v_cvt_pk_bf16_f32 v133, v134, v135\n\t" \
  "v_rcp_f32 v140, v140\n\t" \
  "v_rcp_f32 v141, v141\n\t" \
  "v_rcp_f32 v142, v142\n\t" \
  "v_rcp_f32 v143, v143\n\t" \
  "v_mfma_f32_32x32x16_bf16 v[116:131], v[24:27], v[172:175], v[116:131]\n\t" \
  "v_mul_f32 v136, v104, v136\n\t" \
  "v_mul_f32 v137, v105, v137\n\t" \
  "v_mul_f32 v138, v106, v138\n\t" \
  "v_mul_f32 v139, v107, v139\n\t" \
  "v_cvt_pk_bf16_f32 v134, v136, v137\n\t" \
  "v_cvt_pk_bf16_f32 v135, v138, v139\n\t" \
  "v_mfma_f32_32x32x16_bf16 v[116:131], v[32:35], v[176:179], v[116:131]\n\t" \
  "v_rcp_f32 v144, v144\n\t" \
  "v_rcp_f32 v145, v145\n\t" \
  "v_rcp_f32 v146, v146\n\t" \
  "v_rcp_f32 v147, v147\n\t" \
  "s_waitcnt lgkmcnt(1)\n\t" \
  "v_mfma_f32_16x16x32_bf16 v[84:87], v[200:203], v[192:195], v[84:87]\n\t" \
  "v_mul_f32 v140, v108, v140\n\t" \
  "v_mul_f32 v141, v109, v141\n\t" \
  "v_mul_f32 v142, v110, v142\n\t" \
  "v_mul_f32 v143, v111, v143\n\t" \
  "v_cvt_pk_bf16_f32 v136, v140, v141\n\t" \
  "v_cvt_pk_bf16_f32 v137, v142, v143\n\t" \
  "s_waitcnt lgkmcnt(0)\n\t" \
  "v_mfma_f32_16x16x32_bf16 v[84:87], v[204:207], v[196:199], v[84:87]\n\t" \
  "ds_write_b128 v242, v[132:135] offset:0\n\t" \
  "ds_read_b128 v[92:95], v246 offset:2048\n\t" \
  "v_mul_f32 v144, v112, v144\n\t" \
  "v_mul_f32 v145, v113, v145\n\t" \
  "v_mul_f32 v146, v114, v146\n\t" \
  "v_mul_f32 v147, v115, v147\n\t" \
  "v_cvt_pk_bf16_f32 v138, v144, v145\n\t" \
  "v_cvt_pk_bf16_f32 v139, v146, v147\n\t" \
  "ds_write_b128 v242, v[136:139] offset:16\n\t" \
  "ds_read_b64_tr_b16 v[200:201], v243 offset:0\n\t" \
  "ds_read_b64_tr_b16 v[202:203], v243 offset:320\n\t" \
  "ds_read_b64_tr_b16 v[204:205], v243 offset:1280\n\t" \
  "ds_read_b64_tr_b16 v[206:207], v243 offset:1600\n\t" \
  "ds_read_b128 v[192:195], v241 offset:5376\n\t" \
  "ds_read_b128 v[196:199], v241 offset:5408\n\t" \
  "ds_write_b16 v229, v254 offset:5376\n\t" \
  "ds_read_b128 v[96:99], v246 offset:3072\n\t" \
  "s_sub_u32 %[nt], %[nt], 1\n\t" \
  "s_cmp_lg_u32 %[nt], %[thr]\n\t" \
  "s_cbranch_scc1 3f\n\t" \
  "s_setprio 0\n\t" \
  "3:\n\t" \
  "s_cmp_lt_i32 %[nt], 1\n\t" \
  "s_cselect_b64 vcc, -1, 0\n\t" \
  "v_exp_f32 v148, v116\n\t" \
  "v_exp_f32 v149, v117\n\t" \
  "v_exp_f32 v150, v118\n\t" \
  "v_exp_f32 v151, v119\n\t" \
  "s_waitcnt vmcnt(0)\n\t" \
  "ds_read_b128 v[164:167], v232\n\t" \
  "ds_read_b128 v[168:171], v233\n\t" \
  "ds_read_b128 v[172:175], v234\n\t" \
  "ds_read_b128 v[176:179], v235\n\t" \
  "s_waitcnt lgkmcnt(11)\n\t" \
  "ds_read_b128 v[180:183], v236 offset:4096\n\t" \
  "ds_read_u16_d16_hi v210, v238 offset:5120\n\t" \
  "ds_read_u16_d16_hi v211, v237 offset:5126\n\t" \
  "v_add_u32 v230, v247, v228\n\t" \
  "v_add_u32 v231, v239, v228\n\t" \
  "ds_read_b64 v[208:209], v230\n\t" \
  "s_waitcnt lgkmcnt(11)\n\t" \
  "ds_read_b128 v[188:191], v231 offset:32\n\t" \
  "ds_read_b128 v[184:187], v231\n\t" \
  "v_mfma_f32_16x16x32_bf16 v[88:91], v[92:95], v[132:135], 0\n\t" \
  "v_exp_f32 v152, v120\n\t" \
  "v_exp_f32 v153, v121\n\t" \
  "v_exp_f32 v154, v122\n\t" \
  "v_exp_f32 v155, v123\n\t" \
  "v_mfma_f32_32x32x16_bf16 v[52:67], v[200:203], v[192:195], v[52:67]\n\t" \
  "s_add_u32 m0, %[b1], 4096\n\t" \
  "v_exp_f32 v156, v124\n\t" \
  "v_exp_f32 v157, v125\n\t" \
  "v_exp_f32 v158, v126\n\t" \
  "v_exp_f32 v159, v127\n\t" \
  "s_waitcnt lgkmcnt(10)\n\t" \
  "v_mfma_f32_16x16x32_bf16 v[88:91], v[96:99], v[136:139], v[88:91]\n\t" \
  "s_waitcnt lgkmcnt(3)\n\t" \
  "s_cbranch_vccnz 41f\n\t" \
  "buffer_load_dwordx4 v[224:225], %[rse], 0 idxen offen lds\n\t" \
  "buffer_load_dword v[226:227], %[rsp], 0 idxen offen offset:1024 lds\n\t" \
  "41:\n\t" \
  "s_mov_b32 m0, %[b1]\n\t" \
  "ds_read_b128 v[96:99], v246 offset:0\n\t" \
  "v_exp_f32 v160, v128\n\t" \
  "v_exp_f32 v161, v129\n\t" \
  "v_exp_f32 v162, v130\n\t" \
  "v_exp_f32 v163, v131\n\t" \
  "v_mfma_f32_32x32x16_bf16 v[52:67], v[204:207], v[196:199], v[52:67]\n\t" \
  "v_add_u32 v229, v240, v228\n\t" \
  "ds_write_b16_d16_hi v229, v254 offset:5376\n\t" \
  "v_mfma_f32_32x32x8bf16_1k v[148:163], v[252:253], v[252:253], v[148:163]\n\t" \
  "s_waitcnt lgkmcnt(2)\n\t" \
  "v_mfma_f32_32x32x16_bf16 v[100:115], v[36:39], v[184:187], 0\n\t" \
  "ds_read_u16 v228, v250\n\t" \
  "v_add_u32 v250, 0x100, v250\n\t" \
  "v_sub_f32 v212, v208, v210\n\t" \
  "v_sub_f32 v213, v209, v211\n\t" \
  "v_mul_f32 v208, v212, v212\n\t" \
  "v_mul_f32 v209, v213, v213\n\t" \
  "v_cvt_pk_bf16_f32 v191, v209, v208\n\t" \
  "v_add_u32 v248, 0x100, v248\n\t" \
  "v_add_u32 v249, 0x100, v249\n\t" \
  "v_mfma_f32_32x32x16_bf16 v[100:115], v[4:7], v[164:167], v[100:115]\n\t" \
  "v_rcp_f32 v148, v148\n\t" \
  "v_rcp_f32 v149, v149\n\t" \
  "v_rcp_f32 v150, v150\n\t" \
  "v_rcp_f32 v151, v151\n\t" \
  "v_mfma_f32_32x32x16_bf16 v[100:115], v[12:15], v[168:171], v[100:115]\n\t" \
  "v_rcp_f32 v152, v152\n\t" \
  "v_rcp_f32 v153, v153\n\t" \
  "v_rcp_f32 v154, v154\n\t" \
  "v_rcp_f32 v155, v155\n\t" \
  "v_mfma_f32_32x32x16_bf16 v[100:115], v[20:23], v[172:175], v[100:115]\n\t" \
  "s_cbranch_vccnz 42f\n\t" \
  "buffer_load_dwordx4 v[216:217], %[rsh], 0 idxen offen  lds\n\t" \
  "buffer_load_dwordx4 v[218:219], %[rsh], 0 idxen offen offset:1024 lds\n\t" \
  "buffer_load_dwordx4 v[220:221], %[rsh], 0 idxen offen offset:2048 lds\n\t" \
  "buffer_load_dwordx4 v[222:223], %[rsh], 0 idxen offen offset:3072 lds\n\t" \
  "42:\n\t" \
  "v_mul_f32 v148, v116, v148\n\t" \
  "v_mul_f32 v149, v117, v149\n\t" \
  "v_mul_f32 v150, v118, v150\n\t" \
  "v_mul_f32 v151, v119, v151\n\t" \
  "v_cvt_pk_bf16_f32 v148, v148, v149\n\t" \
  "v_cvt_pk_bf16_f32 v149, v150, v151\n\t" \
  "v_rcp_f32 v156, v156\n\t" \
  "v_rcp_f32 v157, v157\n\t" \
  "v_rcp_f32 v158, v158\n\t" \
  "v_rcp_f32 v159, v159\n\t" \
  "v_mfma_f32_32x32x16_bf16 v[100:115], v[28:31], v[176:179], v[100:115]\n\t" \
  "v_mul_f32 v152, v120, v152\n\t" \
  "v_mul_f32 v153, v121, v153\n\t" \
  "v_mul_f32 v154, v122, v154\n\t" \
  "v_mul_f32 v155, v123, v155\n\t" \
  "v_cvt_pk_bf16_f32 v150, v152, v153\n\t" \
  "v_cvt_pk_bf16_f32 v151, v154, v155\n\t" \
  "s_waitcnt lgkmcnt(2)\n\t" \
  "v_mfma_f32_32x32x16_bf16 v[100:115], v[96:99], v[180:183], v[100:115]\n\t" \
  "v_rcp_f32 v160, v160\n\t" \
  "v_rcp_f32 v161, v161\n\t" \
  "v_rcp_f32 v162, v162\n\t" \
  "v_rcp_f32 v163, v163\n\t" \
  "ds_write_b128 v242, v[148:151] offset:0\n\t" \
  "ds_read_b128 v[92:95], v246 offset:4096\n\t" \
  "v_mul_f32 v156, v124, v156\n\t" \
  "v_mul_f32 v157, v125, v157\n\t" \
  "v_mul_f32 v158, v126, v158\n\t" \
  "v_mul_f32 v159, v127, v159\n\t" \
  "v_cvt_pk_bf16_f32 v152, v156, v157\n\t" \
  "v_cvt_pk_bf16_f32 v153, v158, v159\n\t" \
  "v_mfma_f32_32x32x16_bf16 v[100:115], v[44:47], v[188:191], v[100:115]\n\t" \
  "v_mul_f32 v160, v128, v160\n\t" \
  "v_mul_f32 v161, v129, v161\n\t" \
  "v_mul_f32 v162, v130, v162\n\t" \
  "v_mul_f32 v163, v131, v163\n\t" \
  "v_cvt_pk_bf16_f32 v154, v160, v161\n\t" \
  "v_cvt_pk_bf16_f32 v155, v162, v163\n\t" \
  "s_cmp_lg_u32 %[nt], 0\n\t" \
  "s_cbranch_scc1 1b\n\t" \
  "5:\n\t" \
  "s_waitcnt lgkmcnt(0)\n\t" \
  "s_cmp_lg_u32 %[last], 0\n\t" \
  "s_cbranch_scc1 8f\n\t" \
  "ds_write_b128 v242, v[152:155] offset:16\n\t" \
  "ds_read_b64_tr_b16 v[200:201], v243 offset:0\n\t" \
  "ds_read_b64_tr_b16 v[202:203], v243 offset:320\n\t" \
  "ds_read_b64_tr_b16 v[204:205], v243 offset:1280\n\t" \
  "ds_read_b64_tr_b16 v[206:207], v243 offset:1600\n\t" \
  "ds_read_b128 v[96:99], v246 offset:5120\n\t" \
  "v_mfma_f32_16x16x32_bf16 v[88:91], v[92:95], v[148:151], v[88:91]\n\t" \
  "s_waitcnt lgkmcnt(3)\n\t" \
  "v_mfma_f32_32x32x16_bf16 v[68:83], v[200:203], v[192:195], v[68:83]\n\t" \
  "s_waitcnt lgkmcnt(0)\n\t" \
  "v_mfma_f32_16x16x32_bf16 v[88:91], v[96:99], v[152:155], v[88:91]\n\t" \
  "v_mfma_f32_32x32x16_bf16 v[68:83], v[204:207], v[196:199], v[68:83]\n\t" \
  "s_nop 6\n\t" \
  "v_mul_f32 v214, v214, v88\n\t" \
  "v_mul_f32 v215, v215, v88\n\t" \
  "v_cvt_pk_bf16_f32 v214, v214, v215\n\t" \
  "ds_write_b16 v244, v214 offset:7456\n\t" \
  "ds_write_b16_d16_hi v244, v214 offset:7520\n\t" \
  "ds_read_b128 v[200:203], v245 offset:7456\n\t" \
  "ds_read_b128 v[204:207], v245 offset:7488\n\t" \
  "s_waitcnt lgkmcnt(1)\n\t" \
  "v_mfma_f32_16x16x32_bf16 v[84:87], v[200:203], v[192:195], v[84:87]\n\t" \
  "s_waitcnt lgkmcnt(0)\n\t" \
  "v_mfma_f32_16x16x32_bf16 v[84:87], v[204:207], v[196:199], v[84:87]\n\t" \
  "s_branch 9f\n\t" \
  "6:\n\t" \
  "s_cmp_lg_u32 %[last], 0\n\t" \
  "s_cbranch_scc0 9f\n\t" \
  "s_waitcnt lgkmcnt(0)\n\t" \
  "v_cmp_eq_u32 vcc, 0, v252\n\t" \
  "v_cndmask_b32 v228, 0, 24, vcc\n\t" \
  "v_xor_b32 v216, v232, v228\n\t" \
  "v_xor_b32 v217, v233, v228\n\t" \
  "v_xor_b32 v218, v234, v228\n\t" \
  "v_xor_b32 v219, v235, v228\n\t" \
  "v_xor_b32 v220, 16, v216\n\t" \
  "v_xor_b32 v221, 16, v217\n\t" \
  "v_xor_b32 v222, 16, v218\n\t" \
  "v_xor_b32 v223, 16, v219\n\t" \
  "ds_read_b64 v[164:165], v216\n\t" \
  "ds_read_b64 v[166:167], v220\n\t" \
  "ds_read_b64 v[168:169], v217\n\t" \
  "ds_read_b64 v[170:171], v221\n\t" \
  "ds_read_b64 v[172:173], v218\n\t" \
  "ds_read_b64 v[174:175], v222\n\t" \
  "ds_read_b64 v[176:177], v219\n\t" \
  "ds_read_b64 v[178:179], v223\n\t" \
  "ds_read_b128 v[4:7], v246 offset:6144\n\t" \
  "ds_read_b128 v[8:11], v246 offset:7168\n\t" \
  "ds_read_b128 v[12:15], v246 offset:8192\n\t" \
  "ds_read_b128 v[16:19], v246 offset:9216\n\t" \
  "ds_read_b128 v[20:23], v246 offset:10240\n\t" \
  "ds_read_b128 v[24:27], v246 offset:11264\n\t" \
  "v_subrev_u32 v224, %[b1], v237\n\t" \
  "v_lshl_add_u32 v224, v224, 3, v242\n\t" \
  "v_sub_u32 v224, v224, v228\n\t" \
  "s_nop 4\n\t" \
  "v_cvt_pk_bf16_f32 v132, v52, v53\n\t" \
  "v_cvt_pk_bf16_f32 v133, v54, v55\n\t" \
  "v_cvt_pk_bf16_f32 v134, v56, v57\n\t" \
  "v_cvt_pk_bf16_f32 v135, v58, v59\n\t" \
  "v_cvt_pk_bf16_f32 v136, v60, v61\n\t" \
  "v_cvt_pk_bf16_f32 v137, v62, v63\n\t" \
  "v_cvt_pk_bf16_f32 v138, v64, v65\n\t" \
  "v_cvt_pk_bf16_f32 v139, v66, v67\n\t" \
  "v_cvt_pk_bf16_f32 v36, v68, v69\n\t" \
  "v_cvt_pk_bf16_f32 v37, v70, v71\n\t" \
  "v_cvt_pk_bf16_f32 v38, v72, v73\n\t" \
  "v_cvt_pk_bf16_f32 v39, v74, v75\n\t" \
  "v_cvt_pk_bf16_f32 v40, v76, v77\n\t" \
  "v_cvt_pk_bf16_f32 v41, v78, v79\n\t" \
  "v_cvt_pk_bf16_f32 v42, v80, v81\n\t" \
  "v_cvt_pk_bf16_f32 v43, v82, v83\n\t" \
  "s_waitcnt lgkmcnt(5)\n\t" \
  "v_mfma_f32_32x32x16_bf16 v[100:115], v[4:7], v[164:167], 0\n\t" \
  "s_waitcnt lgkmcnt(4)\n\t" \
  "v_mfma_f32_32x32x16_bf16 v[116:131], v[8:11], v[164:167], 0\n\t" \
  "ds_read_b128 v[28:31], v246 offset:12288\n\t" \
  "ds_read_b128 v[32:35], v246 offset:13312\n\t" \
  "s_waitcnt lgkmcnt(5)\n\t" \
  "v_mfma_f32_32x32x16_bf16 v[100:115], v[12:15], v[168:171], v[100:115]\n\t" \
  "s_waitcnt lgkmcnt(4)\n\t" \
  "v_mfma_f32_32x32x16_bf16 v[116:131], v[16:19], v[168:171], v[116:131]\n\t" \
  "ds_read_b128 v[4:7], v246 offset:14336\n\t" \
  "ds_read_b128 v[8:11], v246 offset:15360\n\t" \
  "s_waitcnt lgkmcnt(5)\n\t" \
  "v_mfma_f32_32x32x16_bf16 v[100:115], v[20:23], v[172:175], v[100:115]\n\t" \
  "s_waitcnt lgkmcnt(4)\n\t" \
  "v_mfma_f32_32x32x16_bf16 v[116:131], v[24:27], v[172:175], v[116:131]\n\t" \
  "ds_read_b128 v[12:15], v246 offset:16384\n\t" \
  "ds_read_b128 v[16:19], v246 offset:17408\n\t" \
  "s_waitcnt lgkmcnt(5)\n\t" \
  "v_mfma_f32_32x32x16_bf16 v[100:115], v[28:31], v[176:179], v[100:115]\n\t" \
  "s_waitcnt lgkmcnt(4)\n\t" \
  "v_mfma_f32_32x32x16_bf16 v[116:131], v[32:35], v[176:179], v[116:131]\n\t" \
  "ds_read_b128 v[20:23], v246 offset:18432\n\t" \
  "ds_read_b128 v[24:27], v246 offset:19456\n\t" \
  "s_waitcnt lgkmcnt(5)\n\t" \
  "v_mfma_f32_32x32x16_bf16 v[100:115], v[4:7], v[132:135], v[100:115]\n\t" \
  "ds_read_b128 v[28:31], v246 offset:20480\n\t" \
  "ds_read_b128 v[32:35], v246 offset:21504\n\t" \
  "s_waitcnt lgkmcnt(5)\n\t" \
  "v_mfma_f32_32x32x16_bf16 v[100:115], v[12:15], v[136:139], v[100:115]\n\t" \
  "s_waitcnt lgkmcnt(3)\n\t" \
  "v_mfma_f32_32x32x16_bf16 v[100:115], v[20:23], v[36:39], v[100:115]\n\t" \
  "s_waitcnt lgkmcnt(1)\n\t" \
  "v_mfma_f32_32x32x16_bf16 v[100:115], v[28:31], v[40:43], v[100:115]\n\t" \
  "s_waitcnt lgkmcnt(0)\n\t" \
  "s_branch 10f\n\t" \
  "8:\n\t" \
  "ds_write_b128 v242, v[152:155] offset:16\n\t" \
  "ds_read_b64_tr_b16 v[200:201], v243 offset:0\n\t" \
  "ds_read_b64_tr_b16 v[202:203], v243 offset:320\n\t" \
  "ds_read_b64_tr_b16 v[204:205], v243 offset:1280\n\t" \
  "ds_read_b64_tr_b16 v[206:207], v243 offset:1600\n\t" \
  "ds_read_b128 v[96:99], v246 offset:5120\n\t" \
  "v_mfma_f32_16x16x32_bf16 v[88:91], v[92:95], v[148:151], v[88:91]\n\t" \
  "s_waitcnt lgkmcnt(0)\n\t" \
  "v_mfma_f32_16x16x32_bf16 v[88:91], v[96:99], v[152:155], v[88:91]\n\t" \
  "v_cmp_eq_u32 vcc, 0, v252\n\t" \
  "v_cndmask_b32 v228, 0, 24, vcc\n\t" \
  "v_xor_b32 v216, v232, v228\n\t" \
  "v_xor_b32 v217, v233, v228\n\t" \
  "v_xor_b32 v218, v234, v228\n\t" \
  "v_xor_b32 v219, v235, v228\n\t" \
  "v_xor_b32 v220, 16, v216\n\t" \
  "v_xor_b32 v221, 16, v217\n\t" \
  "v_xor_b32 v222, 16, v218\n\t" \
  "v_xor_b32 v223, 16, v219\n\t" \
  "ds_read_b64 v[164:165], v216\n\t" \
  "ds_read_b64 v[166:167], v220\n\t" \
  "ds_read_b64 v[168:169], v217\n\t" \
  "ds_read_b64 v[170:171], v221\n\t" \
  "ds_read_b64 v[172:173], v218\n\t" \
  "ds_read_b64 v[174:175], v222\n\t" \
  "ds_read_b64 v[176:177], v219\n\t" \
  "ds_read_b64 v[178:179], v223\n\t" \
  "ds_read_b128 v[4:7], v246 offset:6144\n\t" \
  "ds_read_b128 v[8:11], v246 offset:7168\n\t" \
  "ds_read_b128 v[12:15], v246 offset:8192\n\t" \
  "ds_read_b128 v[16:19], v246 offset:9216\n\t" \
  "ds_read_b128 v[20:23], v246 offset:10240\n\t" \
  "ds_read_b128 v[24:27], v246 offset:11264\n\t" \
  "v_subrev_u32 v224, %[b1], v237\n\t" \
  "v_lshl_add_u32 v224, v224, 3, v242\n\t" \
  "v_sub_u32 v224, v224, v228\n\t" \
  "v_mfma_f32_32x32x16_bf16 v[68:83], v[200:203], v[192:195], v[68:83]\n\t" \
  "s_waitcnt lgkmcnt(5)\n\t" \
  "v_mfma_f32_32x32x16_bf16 v[100:115], v[4:7], v[164:167], 0\n\t" \
  "v_mfma_f32_32x32x16_bf16 v[68:83], v[204:207], v[196:199], v[68:83]\n\t" \
  "s_waitcnt lgkmcnt(4)\n\t" \
  "v_mfma_f32_32x32x16_bf16 v[116:131], v[8:11], v[164:167], 0\n\t" \
  "ds_read_b128 v[28:31], v246 offset:12288\n\t" \
  "ds_read_b128 v[32:35], v246 offset:13312\n\t" \
  "s_waitcnt lgkmcnt(5)\n\t" \
  "v_mfma_f32_32x32x16_bf16 v[100:115], v[12:15], v[168:171], v[100:115]\n\t" \
  "v_mul_f32 v214, v214, v88\n\t" \
  "v_mul_f32 v215, v215, v88\n\t" \
  "v_cvt_pk_bf16_f32 v214, v214, v215\n\t" \
  "ds_write_b16 v244, v214 offset:7456\n\t" \
  "ds_write_b16_d16_hi v244, v214 offset:7520\n\t" \
  "ds_read_b128 v[200:203], v245 offset:7456\n\t" \
  "ds_read_b128 v[204:207], v245 offset:7488\n\t" \
  "v_cvt_pk_bf16_f32 v132, v52, v53\n\t" \
  "v_cvt_pk_bf16_f32 v133, v54, v55\n\t" \
  "v_cvt_pk_bf16_f32 v134, v56, v57\n\t" \
  "v_cvt_pk_bf16_f32 v135, v58, v59\n\t" \
  "s_waitcnt lgkmcnt(8)\n\t" \
  "v_mfma_f32_32x32x16_bf16 v[116:131], v[16:19], v[168:171], v[116:131]\n\t" \
  "ds_read_b128 v[4:7], v246 offset:14336\n\t" \
  "ds_read_b128 v[8:11], v246 offset:15360\n\t" \
  "s_waitcnt lgkmcnt(9)\n\t" \
  "v_mfma_f32_32x32x16_bf16 v[100:115], v[20:23], v[172:175], v[100:115]\n\t" \
  "v_cvt_pk_bf16_f32 v136, v60, v61\n\t" \
  "v_cvt_pk_bf16_f32 v137, v62, v63\n\t" \
  "v_cvt_pk_bf16_f32 v138, v64, v65\n\t" \
  "v_cvt_pk_bf16_f32 v139, v66, v67\n\t" \
  "s_waitcnt lgkmcnt(8)\n\t" \
  "v_mfma_f32_32x32x16_bf16 v[116:131], v[24:27], v[172:175], v[116:131]\n\t" \
  "ds_read_b128 v[12:15], v246 offset:16384\n\t" \
  "ds_read_b128 v[16:19], v246 offset:17408\n\t" \
  "s_waitcnt lgkmcnt(9)\n\t" \
  "v_mfma_f32_32x32x16_bf16 v[100:115], v[28:31], v[176:179], v[100:115]\n\t" \
  "v_cvt_pk_bf16_f32 v36, v68, v69\n\t" \
  "v_cvt_pk_bf16_f32 v37, v70, v71\n\t" \
  "v_cvt_pk_bf16_f32 v38, v72, v73\n\t" \
  "v_cvt_pk_bf16_f32 v39, v74, v75\n\t" \
  "s_waitcnt lgkmcnt(5)\n\t" \
  "v_mfma_f32_16x16x32_bf16 v[84:87], v[200:203], v[192:195], v[84:87]\n\t" \
  "v_cvt_pk_bf16_f32 v40, v76, v77\n\t" \
  "v_cvt_pk_bf16_f32 v41, v78, v79\n\t" \
  "v_cvt_pk_bf16_f32 v42, v80, v81\n\t" \
  "v_cvt_pk_bf16_f32 v43, v82, v83\n\t" \
  "v_mfma_f32_32x32x16_bf16 v[116:131], v[32:35], v[176:179], v[116:131]\n\t" \
  "ds_read_b128 v[20:23], v246 offset:18432\n\t" \
  "ds_read_b128 v[24:27], v246 offset:19456\n\t" \
  "s_waitcnt lgkmcnt(6)\n\t" \
  "v_mfma_f32_16x16x32_bf16 v[84:87], v[204:207], v[196:199], v[84:87]\n\t" \
  "s_waitcnt lgkmcnt(5)\n\t" \
  "v_mfma_f32_32x32x16_bf16 v[100:115], v[4:7], v[132:135], v[100:115]\n\t" \
  "ds_read_b128 v[28:31], v246 offset:20480\n\t" \
  "ds_read_b128 v[32:35], v246 offset:21504\n\t" \
  "s_waitcnt lgkmcnt(5)\n\t" \
  "v_mfma_f32_32x32x16_bf16 v[100:115], v[12:15], v[136:139], v[100:115]\n\t" \
  "s_waitcnt lgkmcnt(3)\n\t" \
  "v_mfma_f32_32x32x16_bf16 v[100:115], v[20:23], v[36:39], v[100:115]\n\t" \
  "s_waitcnt lgkmcnt(1)\n\t" \
  "v_mfma_f32_32x32x16_bf16 v[100:115], v[28:31], v[40:43], v[100:115]\n\t" \
  "s_waitcnt lgkmcnt(0)\n\t" \
  "10:\n\t" \
  "v_mov_b32 v156, 0xbfb8aa3b\n\t" \
  "v_mfma_f32_32x32x16_bf16 v[116:131], v[8:11], v[132:135], v[116:131]\n\t" \
  "v_mfma_f32_32x32x16_bf16 v[116:131], v[16:19], v[136:139], v[116:131]\n\t" \
  "s_nop 8\n\t" \
  "v_exp_f32 v148, v100\n\t" \
  "v_exp_f32 v149, v101\n\t" \
  "v_exp_f32 v150, v102\n\t" \
  "v_exp_f32 v151, v103\n\t" \
  "v_lshlrev_b32 v200, 16, v164\n\t" \
  "v_and_b32 v201, 0xffff0000, v164\n\t" \
  "v_lshlrev_b32 v202, 16, v165\n\t" \
  "v_and_b32 v203, 0xffff0000, v165\n\t" \
  "v_mfma_f32_32x32x16_bf16 v[116:131], v[24:27], v[36:39], v[116:131]\n\t" \
  "v_fmaak_f32 v148, v148, v156, 0xbfb8aa3b\n\t" \
  "v_fmaak_f32 v149, v149, v156, 0xbfb8aa3b\n\t" \
  "v_fmaak_f32 v150, v150, v156, 0xbfb8aa3b\n\t" \
  "v_fmaak_f32 v151, v151, v156, 0xbfb8aa3b\n\t" \
  "v_rcp_f32 v148, v148\n\t" \
  "v_rcp_f32 v149, v149\n\t" \
  "v_rcp_f32 v150, v150\n\t" \
  "v_rcp_f32 v151, v151\n\t" \
  "v_mfma_f32_32x32x16_bf16 v[116:131], v[32:35], v[40:43], v[116:131]\n\t" \
  "v_exp_f32 v152, v104\n\t" \
  "v_exp_f32 v153, v105\n\t" \
  "v_exp_f32 v154, v106\n\t" \
  "v_exp_f32 v155, v107\n\t" \
  "v_fmac_f32 v200, v100, v148\n\t" \
  "v_fmac_f32 v201, v101, v149\n\t" \
  "v_fmac_f32 v202, v102, v150\n\t" \
  "v_fmac_f32 v203, v103, v151\n\t" \
  "v_lshlrev_b32 v204, 16, v166\n\t" \
  "v_and_b32 v205, 0xffff0000, v166\n\t" \
  "v_lshlrev_b32 v206, 16, v167\n\t" \
  "v_and_b32 v207, 0xffff0000, v167\n\t" \
  "v_fmaak_f32 v152, v152, v156, 0xbfb8aa3b\n\t" \
  "v_fmaak_f32 v153, v153, v156, 0xbfb8aa3b\n\t" \
  "v_fmaak_f32 v154, v154, v156, 0xbfb8aa3b\n\t" \
  "v_fmaak_f32 v155, v155, v156, 0xbfb8aa3b\n\t" \
  "v_cvt_pk_bf16_f32 v192, v200, v201\n\t" \
  "v_cvt_pk_bf16_f32 v193, v202, v203\n\t" \
  "ds_write_b64 v224, v[192:193] offset:0\n\t" \
  "v_rcp_f32 v152, v152\n\t" \
  "v_rcp_f32 v153, v153\n\t" \
  "v_rcp_f32 v154, v154\n\t" \
  "v_rcp_f32 v155, v155\n\t" \
  "v_exp_f32 v148, v108\n\t" \
  "v_exp_f32 v149, v109\n\t" \
  "v_exp_f32 v150, v110\n\t" \
  "v_exp_f32 v151, v111\n\t" \
  "v_fmac_f32 v204, v104, v152\n\t" \
  "v_fmac_f32 v205, v105, v153\n\t" \
  "v_fmac_f32 v206, v106, v154\n\t" \
  "v_fmac_f32 v207, v107, v155\n\t" \
  "v_lshlrev_b32 v200, 16, v168\n\t" \
  "v_and_b32 v201, 0xffff0000, v168\n\t" \
  "v_lshlrev_b32 v202, 16, v169\n\t" \
  "v_and_b32 v203, 0xffff0000, v169\n\t" \
  "v_fmaak_f32 v148, v148, v156, 0xbfb8aa3b\n\t" \
  "v_fmaak_f32 v149, v149, v156, 0xbfb8aa3b\n\t" \
  "v_fmaak_f32 v150, v150, v156, 0xbfb8aa3b\n\t" \
  "v_fmaak_f32 v151, v151, v156, 0xbfb8aa3b\n\t" \
  "v_cvt_pk_bf16_f32 v194, v204, v205\n\t" \
  "v_cvt_pk_bf16_f32 v195, v206, v207\n\t" \
  "ds_write_b64 v224, v[194:195] offset:16\n\t" \
  "v_rcp_f32 v148, v148\n\t" \
  "v_rcp_f32 v149, v149\n\t" \
  "v_rcp_f32 v150, v150\n\t" \
  "v_rcp_f32 v151, v151\n\t" \
  "v_exp_f32 v152, v112\n\t" \
  "v_exp_f32 v153, v113\n\t" \
  "v_exp_f32 v154, v114\n\t" \
  "v_exp_f32 v155, v115\n\t" \
  "v_fmac_f32 v200, v108, v148\n\t" \
  "v_fmac_f32 v201, v109, v149\n\t" \
  "v_fmac_f32 v202, v110, v150\n\t" \
  "v_fmac_f32 v203, v111, v151\n\t" \
  "v_lshlrev_b32 v204, 16, v170\n\t" \
  "v_and_b32 v205, 0xffff0000, v170\n\t" \
  "v_lshlrev_b32 v206, 16, v171\n\t" \
  "v_and_b32 v207, 0xffff0000, v171\n\t" \
  "v_fmaak_f32 v152, v152, v156, 0xbfb8aa3b\n\t" \
  "v_fmaak_f32 v153, v153, v156, 0xbfb8aa3b\n\t" \
  "v_fmaak_f32 v154, v154, v156, 0xbfb8aa3b\n\t" \
  "v_fmaak_f32 v155, v155, v156, 0xbfb8aa3b\n\t" \
  "v_cvt_pk_bf16_f32 v196, v200, v201\n\t" \
  "v_cvt_pk_bf16_f32 v197, v202, v203\n\t" \
  "ds_write_b64 v224, v[196:197] offset:32\n\t" \
  "v_rcp_f32 v152, v152\n\t" \
  "v_rcp_f32 v153, v153\n\t" \
  "v_rcp_f32 v154, v154\n\t" \
  "v_rcp_f32 v155, v155\n\t" \
  "v_exp_f32 v148, v116\n\t" \
  "v_exp_f32 v149, v117\n\t" \
  "v_exp_f32 v150, v118\n\t" \
  "v_exp_f32 v151, v119\n\t" \
  "v_fmac_f32 v204, v112, v152\n\t" \
  "v_fmac_f32 v205, v113, v153\n\t" \
  "v_fmac_f32 v206, v114, v154\n\t" \
  "v_fmac_f32 v207, v115, v155\n\t" \
  "v_lshlrev_b32 v200, 16, v172\n\t" \
  "v_and_b32 v201, 0xffff0000, v172\n\t" \
  "v_lshlrev_b32 v202, 16, v173\n\t" \
  "v_and_b32 v203, 0xffff0000, v173\n\t" \
  "v_fmaak_f32 v148, v148, v156, 0xbfb8aa3b\n\t" \
  "v_fmaak_f32 v149, v149, v156, 0xbfb8aa3b\n\t" \
  "v_fmaak_f32 v150, v150, v156, 0xbfb8aa3b\n\t" \
  "v_fmaak_f32 v151, v151, v156, 0xbfb8aa3b\n\t" \
  "v_cvt_pk_bf16_f32 v198, v204, v205\n\t" \
  "v_cvt_pk_bf16_f32 v199, v206, v207\n\t" \
  "ds_write_b64 v224, v[198:199] offset:48\n\t" \
  "v_rcp_f32 v148, v148\n\t" \
  "v_rcp_f32 v149, v149\n\t" \
  "v_rcp_f32 v150, v150\n\t" \
  "v_rcp_f32 v151, v151\n\t" \
  "v_exp_f32 v152, v120\n\t" \
  "v_exp_f32 v153, v121\n\t" \
  "v_exp_f32 v154, v122\n\t" \
  "v_exp_f32 v155, v123\n\t" \
  "v_fmac_f32 v200, v116, v148\n\t" \
  "v_fmac_f32 v201, v117, v149\n\t" \
  "v_fmac_f32 v202, v118, v150\n\t" \
  "v_fmac_f32 v203, v119, v151\n\t" \
  "v_lshlrev_b32 v204, 16, v174\n\t" \
  "v_and_b32 v205, 0xffff0000, v174\n\t" \
  "v_lshlrev_b32 v206, 16, v175\n\t" \
  "v_and_b32 v207, 0xffff0000, v175\n\t" \
  "v_fmaak_f32 v152, v152, v156, 0xbfb8aa3b\n\t" \
  "v_fmaak_f32 v153, v153, v156, 0xbfb8aa3b\n\t" \
  "v_fmaak_f32 v154, v154, v156, 0xbfb8aa3b\n\t" \
  "v_fmaak_f32 v155, v155, v156, 0xbfb8aa3b\n\t" \
  "v_cvt_pk_bf16_f32 v192, v200, v201\n\t" \
  "v_cvt_pk_bf16_f32 v193, v202, v203\n\t" \
  "ds_write_b64 v224, v[192:193] offset:64\n\t" \
  "v_rcp_f32 v152, v152\n\t" \
  "v_rcp_f32 v153, v153\n\t" \
  "v_rcp_f32 v154, v154\n\t" \
  "v_rcp_f32 v155, v155\n\t" \
  "v_exp_f32 v148, v124\n\t" \
  "v_exp_f32 v149, v125\n\t" \
  "v_exp_f32 v150, v126\n\t" \
  "v_exp_f32 v151, v127\n\t" \
  "v_fmac_f32 v204, v120, v152\n\t" \
  "v_fmac_f32 v205, v121, v153\n\t" \
  "v_fmac_f32 v206, v122, v154\n\t" \
  "v_fmac_f32 v207, v123, v155\n\t" \
  "v_lshlrev_b32 v200, 16, v176\n\t" \
  "v_and_b32 v201, 0xffff0000, v176\n\t" \
  "v_lshlrev_b32 v202, 16, v177\n\t" \
  "v_and_b32 v203, 0xffff0000, v177\n\t" \
  "v_fmaak_f32 v148, v148, v156, 0xbfb8aa3b\n\t" \
  "v_fmaak_f32 v149, v149, v156, 0xbfb8aa3b\n\t" \
  "v_fmaak_f32 v150, v150, v156, 0xbfb8aa3b\n\t" \
  "v_fmaak_f32 v151, v151, v156, 0xbfb8aa3b\n\t" \
  "v_cvt_pk_bf16_f32 v194, v204, v205\n\t" \
  "v_cvt_pk_bf16_f32 v195, v206, v207\n\t" \
  "ds_write_b64 v224, v[194:195] offset:80\n\t" \
  "v_rcp_f32 v148, v148\n\t" \
  "v_rcp_f32 v149, v149\n\t" \
  "v_rcp_f32 v150, v150\n\t" \
  "v_rcp_f32 v151, v151\n\t" \
  "v_exp_f32 v152, v128\n\t" \
  "v_exp_f32 v153, v129\n\t" \
  "v_exp_f32 v154, v130\n\t" \
  "v_exp_f32 v155, v131\n\t" \
  "v_fmac_f32 v200, v124, v148\n\t" \
  "v_fmac_f32 v201, v125, v149\n\t" \
  "v_fmac_f32 v202, v126, v150\n\t" \
  "v_fmac_f32 v203, v127, v151\n\t" \
  "v_lshlrev_b32 v204, 16, v178\n\t" \
  "v_and_b32 v205, 0xffff0000, v178\n\t" \
  "v_lshlrev_b32 v206, 16, v179\n\t" \
  "v_and_b32 v207, 0xffff0000, v179\n\t" \
  "v_fmaak_f32 v152, v152, v156, 0xbfb8aa3b\n\t" \
  "v_fmaak_f32 v153, v153, v156, 0xbfb8aa3b\n\t" \
  "v_fmaak_f32 v154, v154, v156, 0xbfb8aa3b\n\t" \
  "v_fmaak_f32 v155, v155, v156, 0xbfb8aa3b\n\t" \
  "v_cvt_pk_bf16_f32 v196, v200, v201\n\t" \
  "v_cvt_pk_bf16_f32 v197, v202, v203\n\t" \
  "ds_write_b64 v224, v[196:197] offset:96\n\t" \
  "v_rcp_f32 v152, v152\n\t" \
  "v_rcp_f32 v153, v153\n\t" \
  "v_rcp_f32 v154, v154\n\t" \
  "v_rcp_f32 v155, v155\n\t" \
  "s_nop 0\n\t" \
  "v_fmac_f32 v204, v128, v152\n\t" \
  "v_fmac_f32 v205, v129, v153\n\t" \
  "v_fmac_f32 v206, v130, v154\n\t" \
  "v_fmac_f32 v207, v131, v155\n\t" \
  "v_cvt_pk_bf16_f32 v198, v204, v205\n\t" \
  "v_cvt_pk_bf16_f32 v199, v206, v207\n\t" \
  "ds_write_b64 v224, v[198:199] offset:112\n\t" \
  "9:\n\t" \
  "ds_write_b16 v229, v254 offset:5376\n\t" \
  "s_mov_b32 m0, %[keep]\n\t" \
  "s_setprio 0\n\t" \
  "s_nop 11\n\t" \
  ""

DI u32x4 tr_frag(unsigned p, int half) {
  const s16x4 lo = __builtin_amdgcn_ds_read_tr16_b64_v4i16((lds_s16x4_p)p);
  const s16x4 hi = __builtin_amdgcn_ds_read_tr16_b64_v4i16((lds_s16x4_p)(p + half));
  return __builtin_bit_cast(u32x4, __builtin_shufflevector(lo, hi, 0, 1, 2, 3, 4, 5, 6, 7));
}
DI u32x4 fold(u32x4 v) {
#pragma unroll
  for (int i = 0; i < 4; i++) v[i] = pk_bf16(bf2f(v[i] & 0xffffu) * -LOG2E, __uint_as_float(v[i] & 0xffff0000u) * -LOG2E);
  return v;
}

__global__ __launch_bounds__(512, 2) void egnn_kernel(const char* __restrict__ hmat, const char* __restrict__ pos,
                                                      const char* __restrict__ ea, const int* __restrict__ src,
                                                      const int* __restrict__ dst, const char* __restrict__ we,
                                                      const char* __restrict__ wx, const char* __restrict__ wh,
                                                      char* __restrict__ ws, uint16_t* __restrict__ out, int N, int E STAMP_ARG) {
  const int w = __builtin_amdgcn_readfirstlane(threadIdx.x >> 6);
  const int bkt = (blockIdx.x & 7) * 32 + (blockIdx.x >> 3);
  const int n0 = bkt * NB;
  STAMP(0);
  unsigned wl = L_WAVE + w * WSZ, mi = L_MI + w * MISZ;
  const auto rs_tab = __builtin_amdgcn_make_buffer_rsrc((void*)(ws + WS_TAB), 0, NBK * NBK * 8, 0x00020000);
  const auto rs_rec = __builtin_amdgcn_make_buffer_rsrc((void*)(ws + WS_REC), 0, NBK * CH * 8, 0x00020000);
  const auto rs_h = __builtin_amdgcn_make_buffer_rsrc((void*)hmat, 0, N * 128, 0x00020000);
  const auto rs_ea = __builtin_amdgcn_make_buffer_rsrc((void*)ea, 0, E * 32, 0x00020000);
  unsigned tag_lo, tag_hi;

  {
    const int lane = lane_id(), tid = w * 64 + lane, r = lane & 31, h = lane >> 5;
    const int e0 = bkt * CH;
    int ne = max(E - e0, 0);
    asm("" : "+s"(ne));
    ne = min(ne, CH);
    const auto rs_d = __builtin_amdgcn_make_buffer_rsrc((void*)(dst + e0), 0, ne * 4, 0x00020000);
    const auto rs_s = __builtin_amdgcn_make_buffer_rsrc((void*)(src + e0), 0, ne * 4, 0x00020000);
    unsigned d[7], sv[7], wdv0, wdv1;
    u32x2 ep, xa, xb;
#pragma unroll
    for (int i = 0; i < 7; i++)
      asm volatile("buffer_load_dword %0, %2, %3, %5 offen nt\n\tbuffer_load_dword %1, %2, %4, %5 offen nt"
                   : "=&v"(d[i]), "=&v"(sv[i]) : "v"(tid * 4), "s"(rs_d), "s"(rs_s), "s"(i * 2048) : "memory");
    LDS(unsigned, L_CNT + tid * 4) = 0;
    barrier_lds();
    asm volatile("global_load_dwordx2 %0, %5, %6 sc1\n\tglobal_load_ushort %1, %7, %8\n\tglobal_load_ushort %2, %7, %8 offset:64\n\t"
                 "global_load_dwordx2 %3, %9, %10\n\tglobal_load_dwordx2 %4, %9, %10 offset:16"
                 : "=&v"(ep), "=&v"(wdv0), "=&v"(wdv1), "=&v"(xa), "=&v"(xb)
                 : "v"(0), "s"(ws + WS_EPOCH), "v"(r * 2), "s"(we + 128 * 128), "v"(h * 8), "s"(wx + ((w + 2) & 3) * 32) : "memory");
    {
      const int j = lane >> 3, jl = (j & 3) * 128 + (lane & 7) * 16, jh = (j >> 2) * 128;
      const char* const h0 = wh + (w < 4 ? 16 * w : 64 + 32 * ((w - 4) >> 1) + 4 * (w & 1)) * 128;
      const char* const e0p = we + (w < 4 ? 64 + 16 * w : 16 * (w - 4)) * 128;
      const unsigned oh = jl + jh * (w < 4 ? 8 : 16), oe = jl + jh * (w < 4 ? 4 : 8);
      const unsigned cc0 = ((lane & 7) ^ (lane >> 4)) * 16, cc1 = ((lane & 7) ^ (4 + (lane >> 4))) * 16;
      const int nr = n0 + w + (lane & 56);
      unsigned keep;
      asm volatile("s_mov_b32 %0, m0\n\ts_mov_b32 m0, %16\n\ts_nop 0\n\t"
                   "global_load_lds_dwordx4 %1, %9\n\tglobal_load_lds_dwordx4 %1, %10 offset:1024\n\t"
                   "global_load_lds_dwordx4 %2, %11 offset:2048\n\tglobal_load_lds_dwordx4 %2, %12 offset:3072\n\t"
                   "s_add_u32 m0, %16, 0x1000\n\ts_nop 0\n\tglobal_load_lds_dwordx4 %3, %13\n\t"
                   "s_mov_b32 m0, %17\n\ts_nop 0\n\tglobal_load_lds_dwordx4 %4, %14\n\t"
                   "s_add_u32 m0, %17, 0x400\n\ts_nop 0\n\tglobal_load_lds_dwordx4 %5, %14\n\t"
                   "s_add_u32 m0, %17, 0x800\n\ts_nop 0\n\tglobal_load_lds_dwordx4 %6, %14\n\t"
                   "s_add_u32 m0, %17, 0xc00\n\ts_nop 0\n\tglobal_load_lds_dwordx4 %7, %14\n\t"
                   "s_add_u32 m0, %17, 0x1000\n\ts_nop 0\n\tglobal_load_lds_dword %8, %15\n\ts_mov_b32 m0, %0"
                   : "=&s"(keep)
                   : "v"(oh), "v"(oe), "v"(w < 2 ? (lane >> 2) * 128 + (lane & 3) * 16 : 0),
                     "v"(min(nr, N - 1) * 128 + cc0), "v"(min(nr + 64, N - 1) * 128 + cc1), "v"(min(nr + 128, N - 1) * 128 + cc0),
                     "v"(min(n0 + w + 192, N - 1) * 128 + cc1), "v"(mul24(min(n0 + w + min(lane >> 1, 24) * 8, N - 1), 6) + (lane & 1) * 2),
                     "s"(h0), "s"(h0 + (w < 4 ? 4 : 8) * 128 - 1024), "s"(e0p - 2048), "s"(e0p + (w < 4 ? 8 : 4) * 128 - 3072),
                     "s"(we + 129 * 128 + (w & 1) * 64), "s"(hmat), "s"(pos), "s"(wl), "s"(mi)
                   : "memory", "scc");
    }
    asm volatile("s_waitcnt vmcnt(15)" : "+v"(d[0]), "+v"(d[1]), "+v"(d[2]), "+v"(d[3]), "+v"(d[4]), "+v"(d[5]), "+v"(d[6]),
                 "+v"(sv[0]), "+v"(sv[1]), "+v"(sv[2]), "+v"(sv[3]), "+v"(sv[4]), "+v"(sv[5]), "+v"(sv[6]) :: "memory");
    STAMP(10);
    unsigned ent;
    auto sort = [&](auto NS_, auto FULL_) __attribute__((always_inline)) {
      constexpr int NS = decltype(NS_)::value;
      constexpr bool FULL = decltype(FULL_)::value;
      unsigned bk[NS], rank[NS];
      u32x2 rec[NS];
#pragma unroll
      for (int i = 0; i < NS; i++) {
        const int el = i * 512 + tid;
        const unsigned b = mul24(d[i], 85599u) >> 24;
        const unsigned dl = d[i] - mul24(b, NB);
        bk[i] = (FULL || el < ne ? b : NBK + lane) * 4;
        rank[i] = __hip_atomic_fetch_add(&LDS(unsigned, L_CNT + bk[i]), 8u, __ATOMIC_RELAXED, __HIP_MEMORY_SCOPE_WORKGROUP);
        rec[i][0] = e0 + el;
        rec[i][1] = ((mul24(dl >> 3, 72u) + dl + 8) << 16) | sv[i];
      }
      LDS(unsigned, L_BASE + (NBK + lane) * 4) = 0x40000000u;
      barrier_lds();
      const u32x4 cv = LDS(u32x4, L_CNT + lane * 16);
      const unsigned sum = cv[0] + cv[1] + cv[2] + cv[3];
      const unsigned ex = wave_scan_incl(sum) - sum + L_REC;
      const u32x4 bv = {ex, ex + cv[0], ex + cv[0] + cv[1], ex + cv[0] + cv[1] + cv[2]};
      LDS(u32x4, L_BASE + lane * 16) = bv;
      unsigned bs[NS];
#pragma unroll
      for (int i = 0; i < NS; i++) bs[i] = LDS(unsigned, L_BASE + bk[i]);
      ent = ((LDS(unsigned, L_BASE + (tid & 255) * 4) - L_REC) >> 3) | (LDS(unsigned, L_CNT + (tid & 255) * 4) << 9);
      const unsigned lim = L_REC + 8 * SEG + lane * 8;
#pragma unroll
      for (int i = 0; i < NS; i++) LDS(u32x2, FULL ? bs[i] + rank[i] : min(bs[i] + rank[i], lim)) = rec[i];
      barrier_lds();
    };
    if (ne == CH && w != 0) sort(std::integral_constant<int, 6>(), std::true_type());
    else sort(std::integral_constant<int, 7>(), std::false_type());
    LDS(unsigned, L_CNT + tid * 4) = 0;
    STAMP(11);
    {
      const auto rs_out = __builtin_amdgcn_make_buffer_rsrc((void*)(ws + WS_REC + (size_t)bkt * (CH * 8)), 0, ((ne + 1) >> 1) * 16, 0x00020000);
#pragma unroll
      for (int i = 0; i < 4; i++)
        __builtin_amdgcn_raw_buffer_store_b128(LDS(u32x4, L_REC + (i * 512 + tid) * 16), rs_out, (i * 512 + tid) * 16, 0, 16);
    }
    asm volatile("s_waitcnt vmcnt(4)" : "+v"(ep), "+v"(wdv0), "+v"(wdv1), "+v"(xa), "+v"(xb) :: "memory");
    {
      const unsigned tp = wl + (8 * h + ((lane >> 2) & 3)) * 128 + ((lane >> 4) & 1) * 32 + (lane & 3) * 8;
      u32x4 f[2], g[2];
#pragma unroll
      for (int t = 0; t < 2; t++) { f[t] = tr_frag(tp + 64 * t, 512); g[t] = tr_frag(tp + 2048 + 64 * t, 512); }
#pragma unroll
      for (int t = 0; t < 2; t++) {
        if (w < 4) { f[t] = fold(f[t]); g[t] = fold(g[t]); }
        LDS(u32x4, L_WH + (2 * w + t) * 1024 + lane * 16) = f[t];
        LDS(u32x4, wl + 2048 + t * 1024 + lane * 16) = g[t];
      }
      u32x4 x;
      if (w < 2) x = fold(tr_frag(wl + W_STE + (8 * h + ((lane >> 2) & 3)) * 64 + ((lane >> 4) & 1) * 32 + (lane & 3) * 8, 256));
      else { const unsigned m = ((lane & 3) == 0 && ((lane >> 2) & 1) == ((lane >> 4) & 1)) ? ~0u : 0u; x = (u32x4){xa[0] & m, xa[1] & m, xb[0] & m, xb[1] & m}; }
      if (w < 6) LDS(u32x4, L_WJ + w * 1024 + lane * 16) = x;
    }
    const unsigned long long e1 = (((unsigned long long)ep[1] << 32) | ep[0]) + 1;
    tag_lo = __builtin_amdgcn_readfirstlane((unsigned)e1 & 0xffu);
    tag_hi = __builtin_amdgcn_readfirstlane((unsigned)(e1 >> 8));
    const u32x2 g = {ent | (tag_lo << 24), tag_hi};
    asm volatile("s_waitcnt vmcnt(0) lgkmcnt(0)\n\ts_barrier" ::: "memory");
    if (tid < NBK) __builtin_amdgcn_raw_buffer_store_b64(g, rs_tab, tid * (NBK * 8) + bkt * 8, 0, 16);
    STAMP(12);
    {
      u32x2 hb[8];
#pragma unroll
      for (int i = 0; i < 8; i++) hb[i] = LDS(u32x2, mi + r * 128 + ((i ^ ((r >> 1) & 7)) * 16) + h * 8);
#pragma unroll
      for (int t = 0; t < 2; t++) {
        f32x16 P;
#pragma unroll
        for (int i = 0; i < 16; i++) P[i] = 0.f;
#pragma unroll
        for (int s = 0; s < 4; s++) {
          const u32x4 a = {hb[2 * s][0], hb[2 * s][1], hb[2 * s + 1][0], hb[2 * s + 1][1]};
          P = MFMA(as_bf(a), LDS(bf16x8, L_WAVE + (4 + s) * WSZ + 2048 + t * 1024 + lane * 16), P);
        }
        P[15] = bf2f(t ? wdv1 : wdv0);
        P[14] = 0.5f * P[15];
#pragma unroll
        for (int i = 0; i < 16; i++) P[i] *= -LOG2E;
#pragma unroll
        for (int s = 0; s < 2; s++) {
          const u32x4 a = {pk_bf16(P[8 * s], P[8 * s + 1]), pk_bf16(P[8 * s + 2], P[8 * s + 3]),
                           pk_bf16(P[8 * s + 4], P[8 * s + 5]), pk_bf16(P[8 * s + 6], P[8 * s + 7])};
          LDS(u32x4, mi + (2 * s + t) * 1024 + lane * 16) = a;
        }
      }
    }
    if (tid >= 256 && tid < 256 + 26 * 4) {
      const int t = tid - 256, row = t >> 2, sx = (t >> 1) & 1, hh = t & 1;
      const int x = row < DUMMY ? row - 16 * sx - 4 * hh : -1;
      u32x4 v;
      v[0] = x == 0 ? 0x3f80u : x == 1 ? 0x3f800000u : 0u;  v[1] = x == 2 ? 0x3f80u : x == 3 ? 0x3f800000u : 0u;
      v[2] = x == 8 ? 0x3f80u : x == 9 ? 0x3f800000u : 0u;  v[3] = x == 10 ? 0x3f80u : x == 11 ? 0x3f800000u : 0u;
      LDS(u32x4, L_LUT + row * 80 + (t & 3) * 16) = v;
    }
    {
      const u32x4 z4 = {0u, 0u, 0u, 0u};
#pragma unroll
      for (int i = 0; i < 3; i++) { const int o = i * 1024 + lane * 16; if (o < 26 * 80 + 320) LDS(u32x4, wl + W_SI + o) = z4; }
      const u32x2 pg = LDS(u32x2, mi + 4096 + r * 8);
      const float pz = __uint_as_float(pg[1] & 0xffff0000u);
      const f32x4 pv = {bf2f(pg[0] & 0xffffu), pz, __uint_as_float(pg[0] & 0xffff0000u), pz};
      if (lane < DUMMY) LDS(f32x4, wl + W_SI + lane * 80 + 64) = pv;
    }
    STAMP(14);
  }

  f32x16 Y0, Y1;
  f32x4 Yp = {0.f, 0.f, 0.f, 0.f};
#pragma unroll
  for (int i = 0; i < 16; i++) { Y0[i] = 0.f; Y1[i] = 0.f; }
  u32x32 W2; u32x16 PA;
  int jbase = 0, part = -1, grp = w, gother = w;

  auto round = [&](bool first) __attribute__((always_inline)) -> bool {
    const int lane = lane_id(), tid = w * 64 + lane, r = lane & 31, h = lane >> 5;
    const unsigned zr = opaque(0);
    if (!first) { LDS(unsigned, L_CNT + tid * 4) = zr; barrier_lds(); }
    LDS(unsigned, L_BASE + lane * 4) = 0x40000000u;
    unsigned tb[8];
    {
      u32x2 t;
      for (int spin = 0;; spin++) {
        asm volatile("buffer_load_dwordx2 %0, %1, %2, %3 offen sc1\n\ts_waitcnt vmcnt(0)"
                     : "=&v"(t) : "v"(w * 256 + r * 8), "s"(rs_tab), "s"(bkt * (NBK * 8)) : "memory");
        if (__builtin_amdgcn_ballot_w64(t[1] != tag_hi || (t[0] >> 24) != tag_lo) == 0) break;
        if (spin > (1 << 20)) { t[0] = 0; break; }
        __builtin_amdgcn_s_sleep(POLL_SLEEP);
      }
      LDS(unsigned, L_WAVE + w * WSZ + W_STE + r * 4) = t[0];
      const u32x4 t0 = LDS(u32x4, L_WAVE + w * WSZ + W_STE + (lane >> 4) * 32), t1 = LDS(u32x4, L_WAVE + w * WSZ + W_STE + (lane >> 4) * 32 + 16);
#pragma unroll
      for (int p = 0; p < 4; p++) { tb[p] = t0[p]; tb[4 + p] = t1[p]; }
    }
    STAMP(8);
    u32x2 rc[10];
    unsigned lmax = 0, nov = 0;
    bool big;
    {
      const unsigned jj = lane & (LG - 1), j = jbase + jj, o0 = mul24((unsigned)tid / LG, 8 * CH * 8) + j * 8, oob = 0x80000000u + zr;
      unsigned off[8], len[8];
#pragma unroll
      for (int p = 0; p < 8; p++) {
        len[p] = (tb[p] >> 12) & 0xfffu;
        if (part >= 0 && part != 2 * p + (w & 1)) len[p] = 0;
        off[p] = o0 + (tb[p] & 0xfffu) * 8;
        const unsigned a = j < len[p] ? off[p] : oob;
        asm volatile("buffer_load_dwordx2 %0, %1, %2, %3 offen sc1" : "=v"(rc[p]) : "v"(a), "s"(rs_rec), "s"(p * CH * 8) : "memory");
      }
      asm volatile("" : "+v"(len[0]), "+v"(len[1]), "+v"(len[2]), "+v"(len[3]), "+v"(len[4]), "+v"(len[5]), "+v"(len[6]), "+v"(len[7]));
      unsigned a9 = oob;
#pragma unroll
      for (int p = 0; p < 8; p++) {
        const unsigned ov = (unsigned)min(max((int)len[p] - (jbase + LG), 0), LG);
        a9 = jj - nov < ov ? off[p] - nov * 8 + (p * CH * 8 + LG * 8) : a9;
        nov += ov;
      }
      asm volatile("buffer_load_dwordx2 %0, %1, %2, 0 offen sc1" : "=v"(rc[8]) : "v"(a9), "s"(rs_rec) : "memory");
#pragma unroll
      for (int p = 0; p < 8; p++) lmax = max(lmax, tb[p] & 0xfff000u);
      rc[9] = (u32x2){zr, zr};
      big = __builtin_amdgcn_ballot_w64(nov > LG) != 0;
      if (big) {
        unsigned a10 = oob, s1 = 0;
#pragma unroll
        for (int p = 0; p < 8; p++) {
          const unsigned ov = (unsigned)min(max((int)len[p] - (jbase + LG), 0), LG);
          a10 = jj + LG - s1 < ov ? off[p] - s1 * 8 + (p * CH * 8 + LG * 16) : a10;
          s1 += ov;
        }
        asm volatile("buffer_load_dwordx2 %0, %1, %2, 0 offen sc1" : "+v"(rc[9]) : "v"(a10), "s"(rs_rec) : "memory");
      }
    }
    unsigned sink = zr;
    if (HPF) {
      const unsigned a = lane < HPF ? (((blockIdx.x >> 3) * 8 + w) * HPF + lane) * 128 : 0x80000000u + zr;
      asm volatile("buffer_load_dword %0, %1, %2, 0 offen" : "+{v3}"(sink) : "v"(a), "s"(rs_h) : "memory");
    }
    STAMP(7);
    if (first) {
#pragma unroll
      for (int i = 0; i < 8; i++) {
        const u32x4 v = LDS(u32x4, L_WAVE + (i >> 1) * WSZ + 2048 + (i & 1) * 1024 + lane * 16);
#pragma unroll
        for (int j = 0; j < 4; j++) W2[4 * i + j] = v[j];
      }
    }
    STAMP(13);
    if ((lmax >> 12) > (unsigned)jbase + 2 * LG || nov > 2 * LG) __hip_atomic_fetch_or(&LDS(unsigned, L_FLAG), nov > 2 * LG ? 3u : 1u, __ATOMIC_RELAXED, __HIP_MEMORY_SCOPE_WORKGROUP);
    unsigned mk1 = (lane & 7) >= 1 ? ~zr : zr, mk2 = (lane & 7) >= 2 ? ~zr : zr, sg0 = L_REC + (lane >> 3) * SEG;
    unsigned lim = L_REC + 8 * SEG + lane * 8, own = n0 + min(8 * (r & 3) + (r >> 2), 24) * 8;
    unsigned ce = ((lane & 1) ^ ((lane >> 4) & 1)) * 16, cp = (lane & 1) * 2;
    unsigned cc0 = ((lane & 7) ^ (lane >> 4)) * 16, cc1 = ((lane & 7) ^ (4 + (lane >> 4))) * 16;
    unsigned q_s4 = L_REC + (lane >> 3) * 32 + 4;
    unsigned q_r2 = L_REC + (4 * ((lane >> 1) & 7) + (lane >> 4)) * 8;
    unsigned q_r1 = L_REC + (4 * (r & 7) + (r >> 3)) * 8 + 4;
    asm volatile("" : "+v"(mk1), "+v"(mk2), "+v"(sg0), "+v"(lim), "+v"(own), "+v"(ce), "+v"(cp), "+v"(cc0), "+v"(cc1), "+v"(q_s4),
                 "+v"(q_r2), "+v"(q_r1));
    asm volatile("s_waitcnt vmcnt(%10)" : "+v"(rc[0]), "+v"(rc[1]), "+v"(rc[2]), "+v"(rc[3]), "+v"(rc[4]), "+v"(rc[5]), "+v"(rc[6]),
                 "+v"(rc[7]), "+v"(rc[8]), "+v"(rc[9]) : "i"(HPF ? 1 : 0) : "memory");
    STAMP(1);
    unsigned rk[10], ky[10];
#pragma unroll
    for (int p = 0; p < 10; p++) {
      if (p == 9 && !big) { ky[p] = 0; rk[p] = 0; break; }
      ky[p] = max((rc[p][1] >> 9) & 0x7fcu, (unsigned)lane * 4);
      rk[p] = __hip_atomic_fetch_add(&LDS(unsigned, L_CNT + ky[p]), 8u + zr, __ATOMIC_RELAXED, __HIP_MEMORY_SCOPE_WORKGROUP);
    }
    if (EPF) {
#pragma unroll
      for (int p = 0; p < 9; p++) {
        const unsigned a = (ky[p] & 0x47cu) - 0x400u < (unsigned)EPF * 4 ? rc[p][0] << 5 : 0x80000000u + zr;
        asm volatile("buffer_load_dword %0, %1, %2, 0 offen" : "+{v3}"(sink) : "v"(a), "s"(rs_ea) : "memory");
      }
    }
    barrier_lds();
    STAMP(2);
    unsigned g;
    const unsigned flagv = LDS(unsigned, L_FLAG);
    {
      const u32x4 cv = LDS(u32x4, L_CNT + 1024 + lane * 16);
      const unsigned sum = cv[0] + cv[1] + cv[2] + cv[3];
      g = sum + (__builtin_amdgcn_update_dpp(0, (int)sum, 0x111, 0xf, 0xf, true) & mk1);
      g += __builtin_amdgcn_update_dpp(0, (int)g, 0x112, 0xf, 0xf, true) & mk2;
      g += __builtin_amdgcn_update_dpp(0, (int)g, 0x114, 0xf, 0xa, false);
      const unsigned ex = g - sum + sg0;
      const u32x4 bv = {ex, ex + cv[0], ex + cv[0] + cv[1], ex + cv[0] + cv[1] + cv[2]};
      LDS(u32x4, L_BASE + 1024 + lane * 16) = bv;
    }
    unsigned bs[9];
#pragma unroll
    for (int p = 0; p < 9; p++) bs[p] = LDS(unsigned, L_BASE + ky[p]);
    const unsigned flag = __builtin_amdgcn_readfirstlane(flagv);
    const bool again = flag & 1;
    const bool over = (flag & 2) || __builtin_amdgcn_ballot_w64(g > (unsigned)(SEG - 256)) != 0;
    if (!over) {
#pragma unroll
      for (int p = 0; p < 9; p++) LDS(u32x2, min(bs[p] + rk[p], lim)) = rc[p];
      if (big) LDS(u32x2, min(LDS(unsigned, L_BASE + ky[9]) + rk[9], lim)) = rc[9];
    }
    if (first) {
      const unsigned key = (g << 3) | (lane >> 3);
      unsigned ks[8], rank = 0;
#pragma unroll
      for (int k = 0; k < 8; k++) ks[k] = __builtin_amdgcn_readlane(key, 8 * k + 7);
#pragma unroll
      for (int k = 0; k < 8; k++) rank += ks[k] > key;
      const unsigned long long sel = 0x8080808080808080ull;
      grp = __builtin_ctzll(__builtin_amdgcn_ballot_w64(rank == (unsigned)(w < 4 ? w : 11 - w)) & sel) >> 3;
      gother = __builtin_ctzll(__builtin_amdgcn_ballot_w64(rank == (unsigned)(w < 4 ? 7 - w : w - 4)) & sel) >> 3;
      wl = L_WAVE + grp * WSZ; mi = L_MI + grp * MISZ;
    }
    unsigned segn = over ? 0u : (unsigned)__builtin_amdgcn_readlane(g, 8 * grp + 7);
    const unsigned seg0 = grp * SEG;
    const int nt = (segn + 255) >> 8;
    {
      const unsigned dum = (DUMMY * 80u + 8 + grp) << 16;
      const u32x2 pad = {zr, dum + zr}, row = {zr, min(own + grp, (unsigned)N - 1) | dum};
      LDS(u32x2, L_REC + seg0 + segn + r * 8) = pad;
      LDS(u32x2, L_REC + seg0 + nt * 256 + r * 8) = row;
    }
    barrier_lds();
    STAMP(3);

    const unsigned o_s4 = q_s4 + seg0, o_r2 = q_r2 + seg0, o_r1 = q_r1 + seg0;
    {
      unsigned ns[4];
#pragma unroll
      for (int i = 0; i < 4; i++) ns[i] = LDS(uint16_t, o_s4 + i * 8);
      const u32x2 nr2 = LDS(u32x2, o_r2);
      dma_tile(hmat, ea, pos, ns[0] * 128 + cc0, ns[1] * 128 + cc1, ns[2] * 128 + cc0, ns[3] * 128 + cc1,
               (nr2[0] << 5) | ce, (nr2[1] & 0xffffu) * 6 + cp, wl);
    }
    if (first) {
#pragma unroll
      for (int i = 0; i < 4; i++) {
        const u32x4 v = LDS(u32x4, mi + i * 1024 + lane * 16);
#pragma unroll
        for (int j = 0; j < 4; j++) PA[4 * i + j] = v[j];
      }
      if (bkt == 0 && tid == 0) {
        const u32x2 e1 = {tag_lo | (tag_hi << 8), tag_hi >> 24};
        asm volatile("global_store_dwordx2 %0, %1, %2 sc1" :: "v"(zr), "v"(e1), "s"(ws + WS_EPOCH) : "memory");
      }
    }
    const unsigned nother = (__builtin_amdgcn_readlane(g, 8 * gother + 7) + 255) >> 8;
    const bool last_round = !over && !again && (part < 0 || part == 15);
    STAMP(4);

    {
      u32x16 A0, B0; u32x8 A1;
      const unsigned bias = 8 + grp;
#pragma unroll
      for (int s = 0; s < 4; s++) A0[s] = wl + W_STH + r * 128 + (((2 * s + h) ^ ((r >> 1) & 7)) * 16);
      A0[4] = wl + r * 32 + ((h ^ ((r >> 3) & 1)) * 16);
      A0[5] = wl + r * 8;
      A0[6] = wl + r * 8 + h * 4;
      A0[7] = L_LUT + h * 16 - bias;
      A0[8] = wl + r * 2 - bias;
      A0[9] = wl + min(r, DUMMY) * 80 + h * 16;
      A0[10] = mi + r * MIL + h * 32;
      A0[11] = mi + (8 * h + ((lane >> 2) & 3)) * MIL + (4 * ((lane >> 4) & 1) + (lane & 3)) * 8;
      A0[12] = wl + h * 128 + r * 2;
      {
        const int c = lane & 3, ok = c < 3 && (lane & 8) == 0 && ((lane >> 2) & 1) == ((lane >> 4) & 1);
        A0[13] = wl + (ok ? (c == 0 ? 0 : c == 1 ? 128 : 64) : 256) + h * 16;
      }
      A0[14] = L_WJ + lane * 16;
      A0[15] = wl + W_SI + 64 + h * 8 - bias;
#pragma unroll
      for (int i = 0; i < 16; i++) B0[i] = zr;
      B0[1] = cc0 + 3072; B0[3] = cc1 + 2048; B0[5] = cc0 + 1024; B0[7] = cc1; B0[9] = ce; B0[11] = cp;
      A1[0] = o_s4 + 256; A1[1] = o_r2 + 256; A1[2] = o_r1 + 2; A1[3] = zr;
      A1[4] = (h ? 0u : 0x3f80u) + zr; A1[5] = zr; A1[6] = 0x3f800000u + zr; A1[7] = zr;
      const auto rsh = __builtin_amdgcn_make_buffer_rsrc((void*)(hmat - 3072), 128, N, 0x00020000);
      const auto rse = __builtin_amdgcn_make_buffer_rsrc((void*)ea, 32, E, 0x00020000);
      const auto rsp = __builtin_amdgcn_make_buffer_rsrc((void*)(pos - 1024), 6, N, 0x00020000);
      unsigned cnt = nt, keep;
#define PRIO_A 14
#define PRIO_B 7
      const int lastv = __builtin_amdgcn_readfirstlane(opaque(last_round ? 1 : 0));
      const int thr = __builtin_amdgcn_readfirstlane(w >= 4 ? max((int)(nother * PRIO_A - cnt * PRIO_B) >> 5, 0) : -1);
      asm volatile(LOOP_ASM
                   : "+{v[52:67]}"(Y0), "+{v[68:83]}"(Y1), "+{v[84:87]}"(Yp), "+{v[216:231]}"(B0), "+{v[232:247]}"(A0), "+{v[248:255]}"(A1),
                     "+{v3}"(sink), [nt] "+s"(cnt), [keep] "=&s"(keep)
                   : "{v[4:35]}"(W2), "{v[36:51]}"(PA), [rsh] "s"(rsh), [rse] "s"(rse), [rsp] "s"(rsp),
                     [b1] "s"(wl), [thr] "s"(thr), [last] "s"(lastv)
                   : "memory", "scc", "vcc", CLOBBER_V);
    }
    if (over) part = 0;
    else if (part >= 0 && part < 15) part++;
    else { part = -1; jbase += 2 * LG; }
    return last_round;
  };
  if (!round(true)) {
    do {
      wait_vm0();
      barrier_lds();
      jbase = __builtin_amdgcn_readfirstlane(jbase); part = __builtin_amdgcn_readfirstlane(part);
    } while (!round(false));
  }
  STAMP(5);

  {
    const int lane = lane_id(), r = lane & 31, h = lane >> 5;
    Yp[0] *= -LN2; Yp[1] *= -LN2; Yp[2] *= -LN2;
    const auto rs_o = __builtin_amdgcn_make_buffer_rsrc((void*)out, 0, N * 128, 0x00020000);
#pragma unroll
    for (int i = 0; i < 4; i++) {
      const int q = 8 * i + (lane >> 3), dl = q * 8 + grp;
      const u32x4 v = LDS(u32x4, mi + q * MIP + (lane & 7) * 16);
      if (dl < NB) __builtin_amdgcn_raw_buffer_store_b128(v, rs_o, (n0 + dl) * 128 + (lane & 7) * 16, 0, OUT_AUX);
    }
    const int dl = r * 8 + grp;
    if (h == 0 && dl < NB && n0 + dl < N) {
      const f32x4 pd = LDS(f32x4, wl + W_SI + r * 80 + 64);
      uint16_t* po = out + (size_t)N * 64 + (size_t)(n0 + dl) * 3;
      const unsigned xy = pk_bf16(pd[0] + Yp[0], pd[2] + Yp[1]), zz = pk_bf16(pd[1] + Yp[2], 0.f);
      po[0] = (uint16_t)xy; po[1] = (uint16_t)(xy >> 16); po[2] = (uint16_t)zz;
    }
  }
  STAMP(6);
}

}

#define STAMP_PASS1
#define STAMP_PASS2

extern "C" void kernel_launch(void* const* d_in, const int* in_sizes, int n_in, void* d_out, int out_size,
                              void* d_ws, size_t ws_size, hipStream_t stream) {
  const int N = in_sizes[0] / 64, E = in_sizes[3];
  if (N > NBK * NB || E > NBK * CH || N < 1 || E < 1) return;
  (void)hipFuncSetAttribute((const void*)egnn_kernel, hipFuncAttributeMaxDynamicSharedMemorySize, L_TOTAL);
  egnn_kernel<<<NBK, 512, L_TOTAL, stream>>>((const char*)d_in[0], (const char*)d_in[1], (const char*)d_in[2], (const int*)d_in[3],
                                             (const int*)d_in[4], (const char*)d_in[5], (const char*)d_in[6], (const char*)d_in[7],
                                             (char*)d_ws, (uint16_t*)d_out, N, E STAMP_PASS2);
}
```

```cpp
#include <hip/hip_runtime.h>
#include <stdint.h>
#include <type_traits>

namespace {

typedef __bf16 bf16x8 __attribute__((ext_vector_type(8)));
typedef __bf16 bf16x2 __attribute__((ext_vector_type(2)));
typedef float f32x16 __attribute__((ext_vector_type(16)));
typedef float f32x4 __attribute__((ext_vector_type(4)));
typedef unsigned u32x4 __attribute__((ext_vector_type(4)));
typedef unsigned u32x2 __attribute__((ext_vector_type(2)));
typedef unsigned u32x8 __attribute__((ext_vector_type(8)));
typedef unsigned u32x16 __attribute__((ext_vector_type(16)));
typedef unsigned u32x32 __attribute__((ext_vector_type(32)));
typedef short s16x4 __attribute__((ext_vector_type(4)));
typedef __attribute__((address_space(3))) s16x4* lds_s16x4_p;

#define DI __device__ __forceinline__
#define MFMA(a, b, c) __builtin_amdgcn_mfma_f32_32x32x16_bf16((a), (b), (c), 0, 0, 0)

#define POLL_SLEEP 2
#define EPF 2
#define OUT_AUX 16
constexpr int NBK = 256;
constexpr int LG = 16;
constexpr int NB = 196;
constexpr int CH = 3136;
constexpr float LOG2E = 1.4426950408889634f;
constexpr float LN2 = 0.6931471805599453f;

constexpr size_t WS_EPOCH = 0;
constexpr size_t WS_TAB = 256;
constexpr size_t WS_REC = WS_TAB + (size_t)NBK * NBK * 8;

constexpr int W_STH = 0, W_STE = 4096, W_STP = 5120, W_SI = 5376, W_TI = W_SI + 26 * 80, WSZ = W_TI + 320;
constexpr int DUMMY = 25;
constexpr int MIP = 144, MISZ = 32 * MIP;
constexpr int MIL = 80;
constexpr int SEG = 17 * 256;
constexpr int L_CNT = 0;
constexpr int L_BASE = L_CNT + 2048;
constexpr int L_FLAG = L_CNT + 512;
constexpr int L_REC = L_BASE + 2048;
constexpr int L_LUT = L_REC + 8 * SEG + 512;
constexpr int L_WAVE = L_LUT + 26 * 80;
constexpr int L_MI = L_WAVE + 8 * WSZ;
constexpr int L_WJ = L_MI + 8 * MISZ;
constexpr int L_WH = L_WJ + 6 * 1024;
constexpr int L_TOTAL = L_WH + 16 * 1024;
static_assert(L_TOTAL <= 163840, "LDS");
static_assert(WSZ % 16 == 0 && L_MI % 16 == 0 && L_LUT % 16 == 0, "align");

#pragma clang diagnostic ignored "-Wint-to-pointer-cast"
#define LDS(T, off) (*(__attribute__((address_space(3))) T*)(unsigned)(off))
DI unsigned mul24(unsigned a, unsigned b) { return (unsigned)__umul24(a, b); }
DI float bf2f(unsigned lo16) { return __uint_as_float(lo16 << 16); }
DI unsigned pk_bf16(float a, float b) { bf16x2 v = {(__bf16)a, (__bf16)b}; return __builtin_bit_cast(unsigned, v); }
DI bf16x8 as_bf(u32x4 v) { return __builtin_bit_cast(bf16x8, v); }
DI int opaque(int x) { asm volatile("" : "+v"(x)); return x; }
DI int lane_id() {
  int x; asm volatile("v_mbcnt_lo_u32_b32 %0, -1, 0\n\tv_mbcnt_hi_u32_b32 %0, -1, %0" : "=v"(x)); return x;
}
DI void wait_vm0() { asm volatile("s_waitcnt vmcnt(0)" ::: "memory"); }
DI void wait_lgkm0() { asm volatile("s_waitcnt lgkmcnt(0)" ::: "memory"); }
DI void barrier_lds() { asm volatile("s_waitcnt lgkmcnt(0)\n\ts_barrier" ::: "memory"); }
DI void dma16(const void* g, unsigned off, unsigned l) {
  unsigned keep;
  asm volatile("s_mov_b32 %0, m0\n\ts_mov_b32 m0, %3\n\ts_nop 0\n\tglobal_load_lds_dwordx4 %1, %2\n\ts_mov_b32 m0, %0"
               : "=&s"(keep) : "v"(off), "s"(g), "s"(l) : "memory");
}
DI void dma4(const void* g, unsigned off, unsigned l) {
  unsigned keep;
  asm volatile("s_mov_b32 %0, m0\n\ts_mov_b32 m0, %3\n\ts_nop 0\n\tglobal_load_lds_dword %1, %2\n\ts_mov_b32 m0, %0"
               : "=&s"(keep) : "v"(off), "s"(g), "s"(l) : "memory");
}
DI void dma_tile(const void* hm, const void* ea, const void* ps, unsigned o0, unsigned o1, unsigned o2, unsigned o3,
                 unsigned oe, unsigned op, unsigned l) {
  unsigned keep;
  asm volatile("s_mov_b32 %0, m0\n\t"
               "s_add_u32 m0, %10, 0x1000\n\ts_nop 0\n\tglobal_load_lds_dwordx4 %5, %8\n\t"
               "s_mov_b32 m0, %10\n\ts_nop 0\n\tglobal_load_lds_dwordx4 %1, %7\n\t"
               "s_add_u32 m0, %10, 0x400\n\ts_nop 0\n\tglobal_load_lds_dwordx4 %2, %7\n\t"
               "s_add_u32 m0, %10, 0x800\n\ts_nop 0\n\tglobal_load_lds_dwordx4 %3, %7\n\t"
               "s_add_u32 m0, %10, 0xc00\n\ts_nop 0\n\tglobal_load_lds_dwordx4 %4, %7\n\t"
               "s_add_u32 m0, %10, 0x1400\n\ts_nop 0\n\tglobal_load_lds_dword %6, %9\n\t"
               "s_mov_b32 m0, %0"
               : "=&s"(keep) : "v"(o0), "v"(o1), "v"(o2), "v"(o3), "v"(oe), "v"(op), "s"(hm), "s"(ea), "s"(ps), "s"(l)
               : "memory", "scc");
}
template <int CTRL, int ROWMASK> DI unsigned dpp_add(unsigned x) {
  return x + (unsigned)__builtin_amdgcn_update_dpp(0, (int)x, CTRL, ROWMASK, 0xf, false);
}
DI unsigned wave_scan_incl(unsigned x) {
  x = dpp_add<0x111, 0xf>(x); x = dpp_add<0x112, 0xf>(x); x = dpp_add<0x114, 0xf>(x); x = dpp_add<0x118, 0xf>(x);
  x = dpp_add<0x142, 0xa>(x); x = dpp_add<0x143, 0xc>(x);
  return x;
}
#define STAMP(i)
#define STAMP_ARG

#define CLOBBER_V "v88", "v89", "v90", "v91", "v92", "v93", "v94", "v95", "v96", "v97", "v98", "v99", "v100", "v101", "v102", "v103", "v104", "v105", "v106", "v107", "v108", "v109", "v110", "v111", "v112", "v113", "v114", "v115", "v116", "v117", "v118", "v119", "v120", "v121", "v122", "v123", "v124", "v125", "v126", "v127", "v128", "v129", "v130", "v131", "v132", "v133", "v134", "v135", "v136", "v137", "v138", "v139", "v140", "v141", "v142", "v143", "v144", "v145", "v146", "v147", "v148", "v149", "v150", "v151", "v152", "v153", "v154", "v155", "v156", "v157", "v158", "v159", "v160", "v161", "v162", "v163", "v164", "v165", "v166", "v167", "v168", "v169", "v170", "v171", "v172", "v173", "v174", "v175", "v176", "v177", "v178", "v179", "v180", "v181", "v182", "v183", "v184", "v185", "v186", "v187", "v188", "v189", "v190", "v191", "v192", "v193", "v194", "v195", "v196", "v197", "v198", "v199", "v200", "v201", "v202", "v203", "v204", "v205", "v206", "v207", "v208", "v209", "v210", "v211", "v212", "v213", "v214", "v215"
#define LOOP_ASM \
  "s_mov_b32 %[keep], m0\n\t" \
  "ds_read_u16 v216, v248 offset:0\n\t" \
  "ds_read_u16 v218, v248 offset:8\n\t" \
  "ds_read_u16 v220, v248 offset:16\n\t" \
  "ds_read_u16 v222, v248 offset:24\n\t" \
  "ds_read_b32 v224, v249\n\t" \
  "ds_read_u16 v226, v249 offset:4\n\t" \
  "ds_read_u16 v228, v250\n\t" \
  "ds_read_b128 v[96:99], v246 offset:0\n\t" \
  "ds_read_b128 v[92:95], v246 offset:1024\n\t" \
  "v_mov_b32 v192, 0\n\t" \
  "v_mov_b32 v193, 0\n\t" \
  "v_mov_b32 v194, 0\n\t" \
  "v_mov_b32 v195, 0\n\t" \
  "v_mov_b32 v196, 0\n\t" \
  "v_mov_b32 v197, 0\n\t" \
  "v_mov_b32 v198, 0\n\t" \
  "v_mov_b32 v199, 0\n\t" \
  "v_mov_b32 v214, 0\n\t" \
  "v_mov_b32 v215, 0\n\t" \
  "v_mov_b32 v88, 0\n\t" \
  "v_add_u32 v248, 0x100, v248\n\t" \
  "v_add_u32 v249, 0x100, v249\n\t" \
  "v_add_u32 v250, 0x100, v250\n\t" \
  "s_waitcnt lgkmcnt(0)\n\t" \
  "v_add_u32 v230, v247, v228\n\t" \
  "v_add_u32 v231, v239, v228\n\t" \
  "ds_read_b64 v[208:209], v230\n\t" \
  "ds_read_b128 v[188:191], v231 offset:32\n\t" \
  "ds_read_b128 v[184:187], v231\n\t" \
  "v_add_u32 v229, v240, v228\n\t" \
  "ds_write_b16_d16_hi v229, v254 offset:5376\n\t" \
  "s_waitcnt vmcnt(0)\n\t" \
  "ds_read_b128 v[164:167], v232\n\t" \
  "ds_read_b128 v[168:171], v233\n\t" \
  "ds_read_b128 v[172:175], v234\n\t" \
  "ds_read_b128 v[176:179], v235\n\t" \
  "ds_read_b128 v[180:183], v236 offset:4096\n\t" \
  "ds_read_u16_d16_hi v210, v238 offset:5120\n\t" \
  "ds_read_u16_d16_hi v211, v237 offset:5126\n\t" \
  "s_waitcnt lgkmcnt(0)\n\t" \
  "s_cmp_lt_i32 %[nt], 1\n\t" \
  "s_cbranch_scc1 7f\n\t" \
  "s_add_u32 m0, %[b1], 4096\n\t" \
  "s_nop 0\n\t" \
  "buffer_load_dwordx4 v[224:225], %[rse], 0 idxen offen lds\n\t" \
  "buffer_load_dword v[226:227], %[rsp], 0 idxen offen offset:1024 lds\n\t" \
  "s_mov_b32 m0, %[b1]\n\t" \
  "s_nop 0\n\t" \
  "buffer_load_dwordx4 v[216:217], %[rsh], 0 idxen offen  lds\n\t" \
  "buffer_load_dwordx4 v[218:219], %[rsh], 0 idxen offen offset:1024 lds\n\t" \
  "buffer_load_dwordx4 v[220:221], %[rsh], 0 idxen offen offset:2048 lds\n\t" \
  "buffer_load_dwordx4 v[222:223], %[rsh], 0 idxen offen offset:3072 lds\n\t" \
  "7:\n\t" \
  "ds_read_u16 v216, v248 offset:0\n\t" \
  "ds_read_u16 v218, v248 offset:8\n\t" \
  "ds_read_u16 v220, v248 offset:16\n\t" \
  "ds_read_u16 v222, v248 offset:24\n\t" \
  "ds_read_b32 v224, v249\n\t" \
  "ds_read_u16 v226, v249 offset:4\n\t" \
  "ds_read_u16 v228, v250\n\t" \
  "v_add_u32 v250, 0x100, v250\n\t" \
  "v_sub_f32 v212, v208, v210\n\t" \
  "v_sub_f32 v213, v209, v211\n\t" \
  "v_mul_f32 v208, v212, v212\n\t" \
  "v_mul_f32 v209, v213, v213\n\t" \
  "v_cvt_pk_bf16_f32 v191, v209, v208\n\t" \
  "v_mfma_f32_32x32x16_bf16 v[100:115], v[36:39], v[184:187], 0\n\t" \
  "v_mfma_f32_32x32x16_bf16 v[100:115], v[4:7], v[164:167], v[100:115]\n\t" \
  "v_mfma_f32_32x32x16_bf16 v[100:115], v[12:15], v[168:171], v[100:115]\n\t" \
  "v_mfma_f32_32x32x16_bf16 v[100:115], v[20:23], v[172:175], v[100:115]\n\t" \
  "v_mfma_f32_32x32x16_bf16 v[100:115], v[28:31], v[176:179], v[100:115]\n\t" \
  "v_mfma_f32_32x32x16_bf16 v[100:115], v[96:99], v[180:183], v[100:115]\n\t" \
  "v_mfma_f32_32x32x16_bf16 v[100:115], v[44:47], v[188:191], v[100:115]\n\t" \
  "s_cmp_eq_u32 %[nt], 0\n\t" \
  "s_cbranch_scc1 6f\n\t" \
  "s_nop 10\n\t" \
  "v_exp_f32 v132, v100\n\t" \
  "v_exp_f32 v133, v101\n\t" \
  "v_exp_f32 v134, v102\n\t" \
  "v_exp_f32 v135, v103\n\t" \
  "v_exp_f32 v136, v104\n\t" \
  "v_exp_f32 v137, v105\n\t" \
  "v_exp_f32 v138, v106\n\t" \
  "v_exp_f32 v139, v107\n\t" \
  "v_exp_f32 v140, v108\n\t" \
  "v_exp_f32 v141, v109\n\t" \
  "v_exp_f32 v142, v110\n\t" \
  "v_exp_f32 v143, v111\n\t" \
  "v_exp_f32 v144, v112\n\t" \
  "v_exp_f32 v145, v113\n\t" \
  "v_exp_f32 v146, v114\n\t" \
  "v_exp_f32 v147, v115\n\t" \
  "s_nop 1\n\t" \
  "v_mfma_f32_32x32x8bf16_1k v[132:147], v[252:253], v[252:253], v[132:147]\n\t" \
  "v_mfma_f32_32x32x16_bf16 v[116:131], v[40:43], v[184:187], 0\n\t" \
  "v_mfma_f32_32x32x16_bf16 v[116:131], v[48:51], v[188:191], v[116:131]\n\t" \
  "s_cmp_lt_i32 %[thr], 0\n\t" \
  "s_cbranch_scc1 2f\n\t" \
  "s_setprio 1\n\t" \
  "s_branch 2f\n\t" \
  "1:\n\t" \
  "ds_write_b128 v242, v[152:155] offset:16\n\t" \
  "ds_read_b64_tr_b16 v[200:201], v243 offset:0\n\t" \
  "ds_read_b64_tr_b16 v[202:203], v243 offset:320\n\t" \
  "ds_read_b64_tr_b16 v[204:205], v243 offset:1280\n\t" \
  "ds_read_b64_tr_b16 v[206:207], v243 offset:1600\n\t" \
  "ds_read_b128 v[96:99], v246 offset:5120\n\t" \
  "v_exp_f32 v132, v100\n\t" \
  "v_exp_f32 v133, v101\n\t" \
  "v_exp_f32 v134, v102\n\t" \
  "v_exp_f32 v135, v103\n\t" \
  "s_waitcnt lgkmcnt(6)\n\t" \
  "v_mfma_f32_16x16x32_bf16 v[88:91], v[92:95], v[148:151], v[88:91]\n\t" \
  "ds_read_b128 v[92:95], v246 offset:1024\n\t" \
  "v_exp_f32 v136, v104\n\t" \
  "v_exp_f32 v137, v105\n\t" \
  "v_exp_f32 v138, v106\n\t" \
  "v_exp_f32 v139, v107\n\t" \
  "s_waitcnt lgkmcnt(4)\n\t" \
  "v_mfma_f32_32x32x16_bf16 v[68:83], v[200:203], v[192:195], v[68:83]\n\t" \
  "ds_read_u16 v216, v248 offset:0\n\t" \
  "ds_read_u16 v218, v248 offset:8\n\t" \
  "ds_read_u16 v220, v248 offset:16\n\t" \
  "ds_read_u16 v222, v248 offset:24\n\t" \
  "ds_read_b32 v224, v249\n\t" \
  "ds_read_u16 v226, v249 offset:4\n\t" \
  "v_exp_f32 v140, v108\n\t" \
  "v_exp_f32 v141, v109\n\t" \
  "v_exp_f32 v142, v110\n\t" \
  "v_exp_f32 v143, v111\n\t" \
  "s_waitcnt lgkmcnt(7)\n\t" \
  "v_mfma_f32_16x16x32_bf16 v[88:91], v[96:99], v[152:155], v[88:91]\n\t" \
  "v_exp_f32 v144, v112\n\t" \
  "v_exp_f32 v145, v113\n\t" \
  "v_exp_f32 v146, v114\n\t" \
  "v_exp_f32 v147, v115\n\t" \
  "v_mfma_f32_32x32x16_bf16 v[68:83], v[204:207], v[196:199], v[68:83]\n\t" \
  "v_mfma_f32_32x32x16_bf16 v[116:131], v[40:43], v[184:187], 0\n\t" \
  "v_mfma_f32_32x32x8bf16_1k v[132:147], v[252:253], v[252:253], v[132:147]\n\t" \
  "v_mfma_f32_32x32x16_bf16 v[116:131], v[48:51], v[188:191], v[116:131]\n\t" \
  "2:\n\t" \
  "v_mul_f32 v214, v214, v88\n\t" \
  "v_mul_f32 v215, v215, v88\n\t" \
  "v_cvt_pk_bf16_f32 v214, v214, v215\n\t" \
  "ds_write_b16 v244, v214 offset:7456\n\t" \
  "ds_write_b16_d16_hi v244, v214 offset:7520\n\t" \
  "ds_read_b128 v[200:203], v245 offset:7456\n\t" \
  "ds_read_b128 v[204:207], v245 offset:7488\n\t" \
  "v_mov_b32 v214, v212\n\t" \
  "v_mov_b32 v215, v213\n\t" \
  "s_waitcnt lgkmcnt(10)\n\t" \
  "v_mfma_f32_32x32x16_bf16 v[116:131], v[92:95], v[180:183], v[116:131]\n\t" \
  "v_rcp_f32 v132, v132\n\t" \
  "v_rcp_f32 v133, v133\n\t" \
  "v_rcp_f32 v134, v134\n\t" \
  "v_rcp_f32 v135, v135\n\t" \
  "v_mfma_f32_32x32x16_bf16 v[116:131], v[8:11], v[164:167], v[116:131]\n\t" \
  "v_rcp_f32 v136, v136\n\t" \
  "v_rcp_f32 v137, v137\n\t" \
  "v_rcp_f32 v138, v138\n\t" \
  "v_rcp_f32 v139, v139\n\t" \
  "v_mfma_f32_32x32x16_bf16 v[116:131], v[16:19], v[168:171], v[116:131]\n\t" \
  "v_mul_f32 v132, v100, v132\n\t" \
  "v_mul_f32 v133, v101, v133\n\t" \
  "v_mul_f32 v134, v102, v134\n\t" \
  "v_mul_f32 v135, v103, v135\n\t" \
  "v_cvt_pk_bf16_f32 v132, v132, v133\n\t" \
  "v_cvt_pk_bf16_f32 v133, v134, v135\n\t" \
  "v_rcp_f32 v140, v140\n\t" \
  "v_rcp_f32 v141, v141\n\t" \
  "v_rcp_f32 v142, v142\n\t" \
  "v_rcp_f32 v143, v143\n\t" \
  "v_mfma_f32_32x32x16_bf16 v[116:131], v[24:27], v[172:175], v[116:131]\n\t" \
  "v_mul_f32 v136, v104, v136\n\t" \
  "v_mul_f32 v137, v105, v137\n\t" \
  "v_mul_f32 v138, v106, v138\n\t" \
  "v_mul_f32 v139, v107, v139\n\t" \
  "v_cvt_pk_bf16_f32 v134, v136, v137\n\t" \
  "v_cvt_pk_bf16_f32 v135, v138, v139\n\t" \
  "v_mfma_f32_32x32x16_bf16 v[116:131], v[32:35], v[176:179], v[116:131]\n\t" \
  "v_rcp_f32 v144, v144\n\t" \
  "v_rcp_f32 v145, v145\n\t" \
  "v_rcp_f32 v146, v146\n\t" \
  "v_rcp_f32 v147, v147\n\t" \
  "s_waitcnt lgkmcnt(1)\n\t" \
  "v_mfma_f32_16x16x32_bf16 v[84:87], v[200:203], v[192:195], v[84:87]\n\t" \
  "v_mul_f32 v140, v108, v140\n\t" \
  "v_mul_f32 v141, v109, v141\n\t" \
  "v_mul_f32 v142, v110, v142\n\t" \
  "v_mul_f32 v143, v111, v143\n\t" \
  "v_cvt_pk_bf16_f32 v136, v140, v141\n\t" \
  "v_cvt_pk_bf16_f32 v137, v142, v143\n\t" \
  "s_waitcnt lgkmcnt(0)\n\t" \
  "v_mfma_f32_16x16x32_bf16 v[84:87], v[204:207], v[196:199], v[84:87]\n\t" \
  "ds_write_b128 v242, v[132:135] offset:0\n\t" \
  "ds_read_b128 v[92:95], v246 offset:2048\n\t" \
  "v_mul_f32 v144, v112, v144\n\t" \
  "v_mul_f32 v145, v113, v145\n\t" \
  "v_mul_f32 v146, v114, v146\n\t" \
  "v_mul_f32 v147, v115, v147\n\t" \
  "v_cvt_pk_bf16_f32 v138, v144, v145\n\t" \
  "v_cvt_pk_bf16_f32 v139, v146, v147\n\t" \
  "ds_write_b128 v242, v[136:139] offset:16\n\t" \
  "ds_read_b64_tr_b16 v[200:201], v243 offset:0\n\t" \
  "ds_read_b64_tr_b16 v[202:203], v243 offset:320\n\t" \
  "ds_read_b64_tr_b16 v[204:205], v243 offset:1280\n\t" \
  "ds_read_b64_tr_b16 v[206:207], v243 offset:1600\n\t" \
  "ds_read_b128 v[192:195], v241 offset:5376\n\t" \
  "ds_read_b128 v[196:199], v241 offset:5408\n\t" \
  "ds_write_b16 v229, v254 offset:5376\n\t" \
  "ds_read_b128 v[96:99], v246 offset:3072\n\t" \
  "s_sub_u32 %[nt], %[nt], 1\n\t" \
  "s_cmp_lg_u32 %[nt], %[thr]\n\t" \
  "s_cbranch_scc1 3f\n\t" \
  "s_setprio 0\n\t" \
  "3:\n\t" \
  "s_cmp_lt_i32 %[nt], 1\n\t" \
  "s_cselect_b64 vcc, -1, 0\n\t" \
  "v_exp_f32 v148, v116\n\t" \
  "v_exp_f32 v149, v117\n\t" \
  "v_exp_f32 v150, v118\n\t" \
  "v_exp_f32 v151, v119\n\t" \
  "s_waitcnt vmcnt(0)\n\t" \
  "ds_read_b128 v[164:167], v232\n\t" \
  "ds_read_b128 v[168:171], v233\n\t" \
  "ds_read_b128 v[172:175], v234\n\t" \
  "ds_read_b128 v[176:179], v235\n\t" \
  "s_waitcnt lgkmcnt(11)\n\t" \
  "ds_read_b128 v[180:183], v236 offset:4096\n\t" \
  "ds_read_u16_d16_hi v210, v238 offset:5120\n\t" \
  "ds_read_u16_d16_hi v211, v237 offset:5126\n\t" \
  "v_add_u32 v230, v247, v228\n\t" \
  "v_add_u32 v231, v239, v228\n\t" \
  "ds_read_b64 v[208:209], v230\n\t" \
  "s_waitcnt lgkmcnt(11)\n\t" \
  "ds_read_b128 v[188:191], v231 offset:32\n\t" \
  "ds_read_b128 v[184:187], v231\n\t" \
  "v_mfma_f32_16x16x32_bf16 v[88:91], v[92:95], v[132:135], 0\n\t" \
  "v_exp_f32 v152, v120\n\t" \
  "v_exp_f32 v153, v121\n\t" \
  "v_exp_f32 v154, v122\n\t" \
  "v_exp_f32 v155, v123\n\t" \
  "v_mfma_f32_32x32x16_bf16 v[52:67], v[200:203], v[192:195], v[52:67]\n\t" \
  "s_add_u32 m0, %[b1], 4096\n\t" \
  "v_exp_f32 v156, v124\n\t" \
  "v_exp_f32 v157, v125\n\t" \
  "v_exp_f32 v158, v126\n\t" \
  "v_exp_f32 v159, v127\n\t" \
  "s_waitcnt lgkmcnt(10)\n\t" \
  "v_mfma_f32_16x16x32_bf16 v[88:91], v[96:99], v[136:139], v[88:91]\n\t" \
  "s_waitcnt lgkmcnt(3)\n\t" \
  "s_cbranch_vccnz 41f\n\t" \
  "buffer_load_dwordx4 v[224:225], %[rse], 0 idxen offen lds\n\t" \
  "buffer_load_dword v[226:227], %[rsp], 0 idxen offen offset:1024 lds\n\t" \
  "41:\n\t" \
  "s_mov_b32 m0, %[b1]\n\t" \
  "ds_read_b128 v[96:99], v246 offset:0\n\t" \
  "v_exp_f32 v160, v128\n\t" \
  "v_exp_f32 v161, v129\n\t" \
  "v_exp_f32 v162, v130\n\t" \
  "v_exp_f32 v163, v131\n\t" \
  "v_mfma_f32_32x32x16_bf16 v[52:67], v[204:207], v[196:199], v[52:67]\n\t" \
  "v_add_u32 v229, v240, v228\n\t" \
  "ds_write_b16_d16_hi v229, v254 offset:5376\n\t" \
  "v_mfma_f32_32x32x8bf16_1k v[148:163], v[252:253], v[252:253], v[148:163]\n\t" \
  "s_waitcnt lgkmcnt(2)\n\t" \
  "v_mfma_f32_32x32x16_bf16 v[100:115], v[36:39], v[184:187], 0\n\t" \
  "ds_read_u16 v228, v250\n\t" \
  "v_add_u32 v250, 0x100, v250\n\t" \
  "v_sub_f32 v212, v208, v210\n\t" \
  "v_sub_f32 v213, v209, v211\n\t" \
  "v_mul_f32 v208, v212, v212\n\t" \
  "v_mul_f32 v209, v213, v213\n\t" \
  "v_cvt_pk_bf16_f32 v191, v209, v208\n\t" \
  "v_add_u32 v248, 0x100, v248\n\t" \
  "v_add_u32 v249, 0x100, v249\n\t" \
  "v_mfma_f32_32x32x16_bf16 v[100:115], v[4:7], v[164:167], v[100:115]\n\t" \
  "v_rcp_f32 v148, v148\n\t" \
  "v_rcp_f32 v149, v149\n\t" \
  "v_rcp_f32 v150, v150\n\t" \
  "v_rcp_f32 v151, v151\n\t" \
  "v_mfma_f32_32x32x16_bf16 v[100:115], v[12:15], v[168:171], v[100:115]\n\t" \
  "v_rcp_f32 v152, v152\n\t" \
  "v_rcp_f32 v153, v153\n\t" \
  "v_rcp_f32 v154, v154\n\t" \
  "v_rcp_f32 v155, v155\n\t" \
  "v_mfma_f32_32x32x16_bf16 v[100:115], v[20:23], v[172:175], v[100:115]\n\t" \
  "s_cbranch_vccnz 42f\n\t" \
  "buffer_load_dwordx4 v[216:217], %[rsh], 0 idxen offen  lds\n\t" \
  "buffer_load_dwordx4 v[218:219], %[rsh], 0 idxen offen offset:1024 lds\n\t" \
  "buffer_load_dwordx4 v[220:221], %[rsh], 0 idxen offen offset:2048 lds\n\t" \
  "buffer_load_dwordx4 v[222:223], %[rsh], 0 idxen offen offset:3072 lds\n\t" \
  "42:\n\t" \
  "v_mul_f32 v148, v116, v148\n\t" \
  "v_mul_f32 v149, v117, v149\n\t" \
  "v_mul_f32 v150, v118, v150\n\t" \
  "v_mul_f32 v151, v119, v151\n\t" \
  "v_cvt_pk_bf16_f32 v148, v148, v149\n\t" \
  "v_cvt_pk_bf16_f32 v149, v150, v151\n\t" \
  "v_rcp_f32 v156, v156\n\t" \
  "v_rcp_f32 v157, v157\n\t" \
  "v_rcp_f32 v158, v158\n\t" \
  "v_rcp_f32 v159, v159\n\t" \
  "v_mfma_f32_32x32x16_bf16 v[100:115], v[28:31], v[176:179], v[100:115]\n\t" \
  "v_mul_f32 v152, v120, v152\n\t" \
  "v_mul_f32 v153, v121, v153\n\t" \
  "v_mul_f32 v154, v122, v154\n\t" \
  "v_mul_f32 v155, v123, v155\n\t" \
  "v_cvt_pk_bf16_f32 v150, v152, v153\n\t" \
  "v_cvt_pk_bf16_f32 v151, v154, v155\n\t" \
  "s_waitcnt lgkmcnt(2)\n\t" \
  "v_mfma_f32_32x32x16_bf16 v[100:115], v[96:99], v[180:183], v[100:115]\n\t" \
  "v_rcp_f32 v160, v160\n\t" \
  "v_rcp_f32 v161, v161\n\t" \
  "v_rcp_f32 v162, v162\n\t" \
  "v_rcp_f32 v163, v163\n\t" \
  "ds_write_b128 v242, v[148:151] offset:0\n\t" \
  "ds_read_b128 v[92:95], v246 offset:4096\n\t" \
  "v_mul_f32 v156, v124, v156\n\t" \
  "v_mul_f32 v157, v125, v157\n\t" \
  "v_mul_f32 v158, v126, v158\n\t" \
  "v_mul_f32 v159, v127, v159\n\t" \
  "v_cvt_pk_bf16_f32 v152, v156, v157\n\t" \
  "v_cvt_pk_bf16_f32 v153, v158, v159\n\t" \
  "v_mfma_f32_32x32x16_bf16 v[100:115], v[44:47], v[188:191], v[100:115]\n\t" \
  "v_mul_f32 v160, v128, v160\n\t" \
  "v_mul_f32 v161, v129, v161\n\t" \
  "v_mul_f32 v162, v130, v162\n\t" \
  "v_mul_f32 v163, v131, v163\n\t" \
  "v_cvt_pk_bf16_f32 v154, v160, v161\n\t" \
  "v_cvt_pk_bf16_f32 v155, v162, v163\n\t" \
  "s_cmp_lg_u32 %[nt], 0\n\t" \
  "s_cbranch_scc1 1b\n\t" \
  "5:\n\t" \
  "s_waitcnt lgkmcnt(0)\n\t" \
  "s_cmp_lg_u32 %[last], 0\n\t" \
  "s_cbranch_scc1 8f\n\t" \
  "ds_write_b128 v242, v[152:155] offset:16\n\t" \
  "ds_read_b64_tr_b16 v[200:201], v243 offset:0\n\t" \
  "ds_read_b64_tr_b16 v[202:203], v243 offset:320\n\t" \
  "ds_read_b64_tr_b16 v[204:205], v243 offset:1280\n\t" \
  "ds_read_b64_tr_b16 v[206:207], v243 offset:1600\n\t" \
  "ds_read_b128 v[96:99], v246 offset:5120\n\t" \
  "v_mfma_f32_16x16x32_bf16 v[88:91], v[92:95], v[148:151], v[88:91]\n\t" \
  "s_waitcnt lgkmcnt(3)\n\t" \
  "v_mfma_f32_32x32x16_bf16 v[68:83], v[200:203], v[192:195], v[68:83]\n\t" \
  "s_waitcnt lgkmcnt(0)\n\t" \
  "v_mfma_f32_16x16x32_bf16 v[88:91], v[96:99], v[152:155], v[88:91]\n\t" \
  "v_mfma_f32_32x32x16_bf16 v[68:83], v[204:207], v[196:199], v[68:83]\n\t" \
  "s_nop 6\n\t" \
  "v_mul_f32 v214, v214, v88\n\t" \
  "v_mul_f32 v215, v215, v88\n\t" \
  "v_cvt_pk_bf16_f32 v214, v214, v215\n\t" \
  "ds_write_b16 v244, v214 offset:7456\n\t" \
  "ds_write_b16_d16_hi v244, v214 offset:7520\n\t" \
  "ds_read_b128 v[200:203], v245 offset:7456\n\t" \
  "ds_read_b128 v[204:207], v245 offset:7488\n\t" \
  "s_waitcnt lgkmcnt(1)\n\t" \
  "v_mfma_f32_16x16x32_bf16 v[84:87], v[200:203], v[192:195], v[84:87]\n\t" \
  "s_waitcnt lgkmcnt(0)\n\t" \
  "v_mfma_f32_16x16x32_bf16 v[84:87], v[204:207], v[196:199], v[84:87]\n\t" \
  "s_branch 9f\n\t" \
  "6:\n\t" \
  "s_cmp_lg_u32 %[last], 0\n\t" \
  "s_cbranch_scc0 9f\n\t" \
  "s_waitcnt lgkmcnt(0)\n\t" \
  "v_cmp_eq_u32 vcc, 0, v252\n\t" \
  "v_cndmask_b32 v228, 0, 24, vcc\n\t" \
  "v_xor_b32 v216, v232, v228\n\t" \
  "v_xor_b32 v217, v233, v228\n\t" \
  "v_xor_b32 v218, v234, v228\n\t" \
  "v_xor_b32 v219, v235, v228\n\t" \
  "v_xor_b32 v220, 16, v216\n\t" \
  "v_xor_b32 v221, 16, v217\n\t" \
  "v_xor_b32 v222, 16, v218\n\t" \
  "v_xor_b32 v223, 16, v219\n\t" \
  "ds_read_b64 v[164:165], v216\n\t" \
  "ds_read_b64 v[166:167], v220\n\t" \
  "ds_read_b64 v[168:169], v217\n\t" \
  "ds_read_b64 v[170:171], v221\n\t" \
  "ds_read_b64 v[172:173], v218\n\t" \
  "ds_read_b64 v[174:175], v222\n\t" \
  "ds_read_b64 v[176:177], v219\n\t" \
  "ds_read_b64 v[178:179], v223\n\t" \
  "ds_read_b128 v[4:7], v246 offset:6144\n\t" \
  "ds_read_b128 v[8:11], v246 offset:7168\n\t" \
  "ds_read_b128 v[12:15], v246 offset:8192\n\t" \
  "ds_read_b128 v[16:19], v246 offset:9216\n\t" \
  "ds_read_b128 v[20:23], v246 offset:10240\n\t" \
  "ds_read_b128 v[24:27], v246 offset:11264\n\t" \
  "v_subrev_u32 v224, %[b1], v237\n\t" \
  "v_lshl_add_u32 v224, v224, 3, v242\n\t" \
  "v_sub_u32 v224, v224, v228\n\t" \
  "s_nop 4\n\t" \
  "v_cvt_pk_bf16_f32 v132, v52, v53\n\t" \
  "v_cvt_pk_bf16_f32 v133, v54, v55\n\t" \
  "v_cvt_pk_bf16_f32 v134, v56, v57\n\t" \
  "v_cvt_pk_bf16_f32 v135, v58, v59\n\t" \
  "v_cvt_pk_bf16_f32 v136, v60, v61\n\t" \
  "v_cvt_pk_bf16_f32 v137, v62, v63\n\t" \
  "v_cvt_pk_bf16_f32 v138, v64, v65\n\t" \
  "v_cvt_pk_bf16_f32 v139, v66, v67\n\t" \
  "v_cvt_pk_bf16_f32 v36, v68, v69\n\t" \
  "v_cvt_pk_bf16_f32 v37, v70, v71\n\t" \
  "v_cvt_pk_bf16_f32 v38, v72, v73\n\t" \
  "v_cvt_pk_bf16_f32 v39, v74, v75\n\t" \
  "v_cvt_pk_bf16_f32 v40, v76, v77\n\t" \
  "v_cvt_pk_bf16_f32 v41, v78, v79\n\t" \
  "v_cvt_pk_bf16_f32 v42, v80, v81\n\t" \
  "v_cvt_pk_bf16_f32 v43, v82, v83\n\t" \
  "s_waitcnt lgkmcnt(5)\n\t" \
  "v_mfma_f32_32x32x16_bf16 v[100:115], v[4:7], v[164:167], 0\n\t" \
  "s_waitcnt lgkmcnt(4)\n\t" \
  "v_mfma_f32_32x32x16_bf16 v[116:131], v[8:11], v[164:167], 0\n\t" \
  "ds_read_b128 v[28:31], v246 offset:12288\n\t" \
  "ds_read_b128 v[32:35], v246 offset:13312\n\t" \
  "s_waitcnt lgkmcnt(5)\n\t" \
  "v_mfma_f32_32x32x16_bf16 v[100:115], v[12:15], v[168:171], v[100:115]\n\t" \
  "s_waitcnt lgkmcnt(4)\n\t" \
  "v_mfma_f32_32x32x16_bf16 v[116:131], v[16:19], v[168:171], v[116:131]\n\t" \
  "ds_read_b128 v[4:7], v246 offset:14336\n\t" \
  "ds_read_b128 v[8:11], v246 offset:15360\n\t" \
  "s_waitcnt lgkmcnt(5)\n\t" \
  "v_mfma_f32_32x32x16_bf16 v[100:115], v[20:23], v[172:175], v[100:115]\n\t" \
  "s_waitcnt lgkmcnt(4)\n\t" \
  "v_mfma_f32_32x32x16_bf16 v[116:131], v[24:27], v[172:175], v[116:131]\n\t" \
  "ds_read_b128 v[12:15], v246 offset:16384\n\t" \
  "ds_read_b128 v[16:19], v246 offset:17408\n\t" \
  "s_waitcnt lgkmcnt(5)\n\t" \
  "v_mfma_f32_32x32x16_bf16 v[100:115], v[28:31], v[176:179], v[100:115]\n\t" \
  "s_waitcnt lgkmcnt(4)\n\t" \
  "v_mfma_f32_32x32x16_bf16 v[116:131], v[32:35], v[176:179], v[116:131]\n\t" \
  "ds_read_b128 v[20:23], v246 offset:18432\n\t" \
  "ds_read_b128 v[24:27], v246 offset:19456\n\t" \
  "s_waitcnt lgkmcnt(5)\n\t" \
  "v_mfma_f32_32x32x16_bf16 v[100:115], v[4:7], v[132:135], v[100:115]\n\t" \
  "ds_read_b128 v[28:31], v246 offset:20480\n\t" \
  "ds_read_b128 v[32:35], v246 offset:21504\n\t" \
  "s_waitcnt lgkmcnt(5)\n\t" \
  "v_mfma_f32_32x32x16_bf16 v[100:115], v[12:15], v[136:139], v[100:115]\n\t" \
  "s_waitcnt lgkmcnt(3)\n\t" \
  "v_mfma_f32_32x32x16_bf16 v[100:115], v[20:23], v[36:39], v[100:115]\n\t" \
  "s_waitcnt lgkmcnt(1)\n\t" \
  "v_mfma_f32_32x32x16_bf16 v[100:115], v[28:31], v[40:43], v[100:115]\n\t" \
  "s_waitcnt lgkmcnt(0)\n\t" \
  "s_branch 10f\n\t" \
  "8:\n\t" \
  "ds_write_b128 v242, v[152:155] offset:16\n\t" \
  "ds_read_b64_tr_b16 v[200:201], v243 offset:0\n\t" \
  "ds_read_b64_tr_b16 v[202:203], v243 offset:320\n\t" \
  "ds_read_b64_tr_b16 v[204:205], v243 offset:1280\n\t" \
  "ds_read_b64_tr_b16 v[206:207], v243 offset:1600\n\t" \
  "ds_read_b128 v[96:99], v246 offset:5120\n\t" \
  "v_mfma_f32_16x16x32_bf16 v[88:91], v[92:95], v[148:151], v[88:91]\n\t" \
  "s_waitcnt lgkmcnt(0)\n\t" \
  "v_mfma_f32_16x16x32_bf16 v[88:91], v[96:99], v[152:155], v[88:91]\n\t" \
  "v_cmp_eq_u32 vcc, 0, v252\n\t" \
  "v_cndmask_b32 v228, 0, 24, vcc\n\t" \
  "v_xor_b32 v216, v232, v228\n\t" \
  "v_xor_b32 v217, v233, v228\n\t" \
  "v_xor_b32 v218, v234, v228\n\t" \
  "v_xor_b32 v219, v235, v228\n\t" \
  "v_xor_b32 v220, 16, v216\n\t" \
  "v_xor_b32 v221, 16, v217\n\t" \
  "v_xor_b32 v222, 16, v218\n\t" \
  "v_xor_b32 v223, 16, v219\n\t" \
  "ds_read_b64 v[164:165], v216\n\t" \
  "ds_read_b64 v[166:167], v220\n\t" \
  "ds_read_b64 v[168:169], v217\n\t" \
  "ds_read_b64 v[170:171], v221\n\t" \
  "ds_read_b64 v[172:173], v218\n\t" \
  "ds_read_b64 v[174:175], v222\n\t" \
  "ds_read_b64 v[176:177], v219\n\t" \
  "ds_read_b64 v[178:179], v223\n\t" \
  "ds_read_b128 v[4:7], v246 offset:6144\n\t" \
  "ds_read_b128 v[8:11], v246 offset:7168\n\t" \
  "ds_read_b128 v[12:15], v246 offset:8192\n\t" \
  "ds_read_b128 v[16:19], v246 offset:9216\n\t" \
  "ds_read_b128 v[20:23], v246 offset:10240\n\t" \
  "ds_read_b128 v[24:27], v246 offset:11264\n\t" \
  "v_subrev_u32 v224, %[b1], v237\n\t" \
  "v_lshl_add_u32 v224, v224, 3, v242\n\t" \
  "v_sub_u32 v224, v224, v228\n\t" \
  "v_mfma_f32_32x32x16_bf16 v[68:83], v[200:203], v[192:195], v[68:83]\n\t" \
  "s_waitcnt lgkmcnt(5)\n\t" \
  "v_mfma_f32_32x32x16_bf16 v[100:115], v[4:7], v[164:167], 0\n\t" \
  "v_mfma_f32_32x32x16_bf16 v[68:83], v[204:207], v[196:199], v[68:83]\n\t" \
  "s_waitcnt lgkmcnt(4)\n\t" \
  "v_mfma_f32_32x32x16_bf16 v[116:131], v[8:11], v[164:167], 0\n\t" \
  "ds_read_b128 v[28:31], v246 offset:12288\n\t" \
  "ds_read_b128 v[32:35], v246 offset:13312\n\t" \
  "s_waitcnt lgkmcnt(5)\n\t" \
  "v_mfma_f32_32x32x16_bf16 v[100:115], v[12:15], v[168:171], v[100:115]\n\t" \
  "v_mul_f32 v214, v214, v88\n\t" \
  "v_mul_f32 v215, v215, v88\n\t" \
  "v_cvt_pk_bf16_f32 v214, v214, v215\n\t" \
  "ds_write_b16 v244, v214 offset:7456\n\t" \
  "ds_write_b16_d16_hi v244, v214 offset:7520\n\t" \
  "ds_read_b128 v[200:203], v245 offset:7456\n\t" \
  "ds_read_b128 v[204:207], v245 offset:7488\n\t" \
  "v_cvt_pk_bf16_f32 v132, v52, v53\n\t" \
  "v_cvt_pk_bf16_f32 v133, v54, v55\n\t" \
  "v_cvt_pk_bf16_f32 v134, v56, v57\n\t" \
  "v_cvt_pk_bf16_f32 v135, v58, v59\n\t" \
  "s_waitcnt lgkmcnt(8)\n\t" \
  "v_mfma_f32_32x32x16_bf16 v[116:131], v[16:19], v[168:171], v[116:131]\n\t" \
  "ds_read_b128 v[4:7], v246 offset:14336\n\t" \
  "ds_read_b128 v[8:11], v246 offset:15360\n\t" \
  "s_waitcnt lgkmcnt(9)\n\t" \
  "v_mfma_f32_32x32x16_bf16 v[100:115], v[20:23], v[172:175], v[100:115]\n\t" \
  "v_cvt_pk_bf16_f32 v136, v60, v61\n\t" \
  "v_cvt_pk_bf16_f32 v137, v62, v63\n\t" \
  "v_cvt_pk_bf16_f32 v138, v64, v65\n\t" \
  "v_cvt_pk_bf16_f32 v139, v66, v67\n\t" \
  "s_waitcnt lgkmcnt(8)\n\t" \
  "v_mfma_f32_32x32x16_bf16 v[116:131], v[24:27], v[172:175], v[116:131]\n\t" \
  "ds_read_b128 v[12:15], v246 offset:16384\n\t" \
  "ds_read_b128 v[16:19], v246 offset:17408\n\t" \
  "s_waitcnt lgkmcnt(9)\n\t" \
  "v_mfma_f32_32x32x16_bf16 v[100:115], v[28:31], v[176:179], v[100:115]\n\t" \
  "v_cvt_pk_bf16_f32 v36, v68, v69\n\t" \
  "v_cvt_pk_bf16_f32 v37, v70, v71\n\t" \
  "v_cvt_pk_bf16_f32 v38, v72, v73\n\t" \
  "v_cvt_pk_bf16_f32 v39, v74, v75\n\t" \
  "s_waitcnt lgkmcnt(5)\n\t" \
  "v_mfma_f32_16x16x32_bf16 v[84:87], v[200:203], v[192:195], v[84:87]\n\t" \
  "v_cvt_pk_bf16_f32 v40, v76, v77\n\t" \
  "v_cvt_pk_bf16_f32 v41, v78, v79\n\t" \
  "v_cvt_pk_bf16_f32 v42, v80, v81\n\t" \
  "v_cvt_pk_bf16_f32 v43, v82, v83\n\t" \
  "v_mfma_f32_32x32x16_bf16 v[116:131], v[32:35], v[176:179], v[116:131]\n\t" \
  "ds_read_b128 v[20:23], v246 offset:18432\n\t" \
  "ds_read_b128 v[24:27], v246 offset:19456\n\t" \
  "s_waitcnt lgkmcnt(6)\n\t" \
  "v_mfma_f32_16x16x32_bf16 v[84:87], v[204:207], v[196:199], v[84:87]\n\t" \
  "s_waitcnt lgkmcnt(5)\n\t" \
  "v_mfma_f32_32x32x16_bf16 v[100:115], v[4:7], v[132:135], v[100:115]\n\t" \
  "ds_read_b128 v[28:31], v246 offset:20480\n\t" \
  "ds_read_b128 v[32:35], v246 offset:21504\n\t" \
  "s_waitcnt lgkmcnt(5)\n\t" \
  "v_mfma_f32_32x32x16_bf16 v[100:115], v[12:15], v[136:139], v[100:115]\n\t" \
  "s_waitcnt lgkmcnt(3)\n\t" \
  "v_mfma_f32_32x32x16_bf16 v[100:115], v[20:23], v[36:39], v[100:115]\n\t" \
  "s_waitcnt lgkmcnt(1)\n\t" \
  "v_mfma_f32_32x32x16_bf16 v[100:115], v[28:31], v[40:43], v[100:115]\n\t" \
  "s_waitcnt lgkmcnt(0)\n\t" \
  "10:\n\t" \
  "v_mov_b32 v156, 0xbfb8aa3b\n\t" \
  "v_mfma_f32_32x32x16_bf16 v[116:131], v[8:11], v[132:135], v[116:131]\n\t" \
  "v_mfma_f32_32x32x16_bf16 v[116:131], v[16:19], v[136:139], v[116:131]\n\t" \
  "s_nop 8\n\t" \
  "v_exp_f32 v148, v100\n\t" \
  "v_exp_f32 v149, v101\n\t" \
  "v_exp_f32 v150, v102\n\t" \
  "v_exp_f32 v151, v103\n\t" \
  "v_lshlrev_b32 v200, 16, v164\n\t" \
  "v_and_b32 v201, 0xffff0000, v164\n\t" \
  "v_lshlrev_b32 v202, 16, v165\n\t" \
  "v_and_b32 v203, 0xffff0000, v165\n\t" \
  "v_mfma_f32_32x32x16_bf16 v[116:131], v[24:27], v[36:39], v[116:131]\n\t" \
  "v_fmaak_f32 v148, v148, v156, 0xbfb8aa3b\n\t" \
  "v_fmaak_f32 v149, v149, v156, 0xbfb8aa3b\n\t" \
  "v_fmaak_f32 v150, v150, v156, 0xbfb8aa3b\n\t" \
  "v_fmaak_f32 v151, v151, v156, 0xbfb8aa3b\n\t" \
  "v_rcp_f32 v148, v148\n\t" \
  "v_rcp_f32 v149, v149\n\t" \
  "v_rcp_f32 v150, v150\n\t" \
  "v_rcp_f32 v151, v151\n\t" \
  "v_mfma_f32_32x32x16_bf16 v[116:131], v[32:35], v[40:43], v[116:131]\n\t" \
  "v_exp_f32 v152, v104\n\t" \
  "v_exp_f32 v153, v105\n\t" \
  "v_exp_f32 v154, v106\n\t" \
  "v_exp_f32 v155, v107\n\t" \
  "v_fmac_f32 v200, v100, v148\n\t" \
  "v_fmac_f32 v201, v101, v149\n\t" \
  "v_fmac_f32 v202, v102, v150\n\t" \
  "v_fmac_f32 v203, v103, v151\n\t" \
  "v_lshlrev_b32 v204, 16, v166\n\t" \
  "v_and_b32 v205, 0xffff0000, v166\n\t" \
  "v_lshlrev_b32 v206, 16, v167\n\t" \
  "v_and_b32 v207, 0xffff0000, v167\n\t" \
  "v_fmaak_f32 v152, v152, v156, 0xbfb8aa3b\n\t" \
  "v_fmaak_f32 v153, v153, v156, 0xbfb8aa3b\n\t" \
  "v_fmaak_f32 v154, v154, v156, 0xbfb8aa3b\n\t" \
  "v_fmaak_f32 v155, v155, v156, 0xbfb8aa3b\n\t" \
  "v_cvt_pk_bf16_f32 v192, v200, v201\n\t" \
  "v_cvt_pk_bf16_f32 v193, v202, v203\n\t" \
  "ds_write_b64 v224, v[192:193] offset:0\n\t" \
  "v_rcp_f32 v152, v152\n\t" \
  "v_rcp_f32 v153, v153\n\t" \
  "v_rcp_f32 v154, v154\n\t" \
  "v_rcp_f32 v155, v155\n\t" \
  "v_exp_f32 v148, v108\n\t" \
  "v_exp_f32 v149, v109\n\t" \
  "v_exp_f32 v150, v110\n\t" \
  "v_exp_f32 v151, v111\n\t" \
  "v_fmac_f32 v204, v104, v152\n\t" \
  "v_fmac_f32 v205, v105, v153\n\t" \
  "v_fmac_f32 v206, v106, v154\n\t" \
  "v_fmac_f32 v207, v107, v155\n\t" \
  "v_lshlrev_b32 v200, 16, v168\n\t" \
  "v_and_b32 v201, 0xffff0000, v168\n\t" \
  "v_lshlrev_b32 v202, 16, v169\n\t" \
  "v_and_b32 v203, 0xffff0000, v169\n\t" \
  "v_fmaak_f32 v148, v148, v156, 0xbfb8aa3b\n\t" \
  "v_fmaak_f32 v149, v149, v156, 0xbfb8aa3b\n\t" \
  "v_fmaak_f32 v150, v150, v156, 0xbfb8aa3b\n\t" \
  "v_fmaak_f32 v151, v151, v156, 0xbfb8aa3b\n\t" \
  "v_cvt_pk_bf16_f32 v194, v204, v205\n\t" \
  "v_cvt_pk_bf16_f32 v195, v206, v207\n\t" \
  "ds_write_b64 v224, v[194:195] offset:16\n\t" \
  "v_rcp_f32 v148, v148\n\t" \
  "v_rcp_f32 v149, v149\n\t" \
  "v_rcp_f32 v150, v150\n\t" \
  "v_rcp_f32 v151, v151\n\t" \
  "v_exp_f32 v152, v112\n\t" \
  "v_exp_f32 v153, v113\n\t" \
  "v_exp_f32 v154, v114\n\t" \
  "v_exp_f32 v155, v115\n\t" \
  "v_fmac_f32 v200, v108, v148\n\t" \
  "v_fmac_f32 v201, v109, v149\n\t" \
  "v_fmac_f32 v202, v110, v150\n\t" \
  "v_fmac_f32 v203, v111, v151\n\t" \
  "v_lshlrev_b32 v204, 16, v170\n\t" \
  "v_and_b32 v205, 0xffff0000, v170\n\t" \
  "v_lshlrev_b32 v206, 16, v171\n\t" \
  "v_and_b32 v207, 0xffff0000, v171\n\t" \
  "v_fmaak_f32 v152, v152, v156, 0xbfb8aa3b\n\t" \
  "v_fmaak_f32 v153, v153, v156, 0xbfb8aa3b\n\t" \
  "v_fmaak_f32 v154, v154, v156, 0xbfb8aa3b\n\t" \
  "v_fmaak_f32 v155, v155, v156, 0xbfb8aa3b\n\t" \
  "v_cvt_pk_bf16_f32 v196, v200, v201\n\t" \
  "v_cvt_pk_bf16_f32 v197, v202, v203\n\t" \
  "ds_write_b64 v224, v[196:197] offset:32\n\t" \
  "v_rcp_f32 v152, v152\n\t" \
  "v_rcp_f32 v153, v153\n\t" \
  "v_rcp_f32 v154, v154\n\t" \
  "v_rcp_f32 v155, v155\n\t" \
  "v_exp_f32 v148, v116\n\t" \
  "v_exp_f32 v149, v117\n\t" \
  "v_exp_f32 v150, v118\n\t" \
  "v_exp_f32 v151, v119\n\t" \
  "v_fmac_f32 v204, v112, v152\n\t" \
  "v_fmac_f32 v205, v113, v153\n\t" \
  "v_fmac_f32 v206, v114, v154\n\t" \
  "v_fmac_f32 v207, v115, v155\n\t" \
  "v_lshlrev_b32 v200, 16, v172\n\t" \
  "v_and_b32 v201, 0xffff0000, v172\n\t" \
  "v_lshlrev_b32 v202, 16, v173\n\t" \
  "v_and_b32 v203, 0xffff0000, v173\n\t" \
  "v_fmaak_f32 v148, v148, v156, 0xbfb8aa3b\n\t" \
  "v_fmaak_f32 v149, v149, v156, 0xbfb8aa3b\n\t" \
  "v_fmaak_f32 v150, v150, v156, 0xbfb8aa3b\n\t" \
  "v_fmaak_f32 v151, v151, v156, 0xbfb8aa3b\n\t" \
  "v_cvt_pk_bf16_f32 v198, v204, v205\n\t" \
  "v_cvt_pk_bf16_f32 v199, v206, v207\n\t" \
  "ds_write_b64 v224, v[198:199] offset:48\n\t" \
  "v_rcp_f32 v148, v148\n\t" \
  "v_rcp_f32 v149, v149\n\t" \
  "v_rcp_f32 v150, v150\n\t" \
  "v_rcp_f32 v151, v151\n\t" \
  "v_exp_f32 v152, v120\n\t" \
  "v_exp_f32 v153, v121\n\t" \
  "v_exp_f32 v154, v122\n\t" \
  "v_exp_f32 v155, v123\n\t" \
  "v_fmac_f32 v200, v116, v148\n\t" \
  "v_fmac_f32 v201, v117, v149\n\t" \
  "v_fmac_f32 v202, v118, v150\n\t" \
  "v_fmac_f32 v203, v119, v151\n\t" \
  "v_lshlrev_b32 v204, 16, v174\n\t" \
  "v_and_b32 v205, 0xffff0000, v174\n\t" \
  "v_lshlrev_b32 v206, 16, v175\n\t" \
  "v_and_b32 v207, 0xffff0000, v175\n\t" \
  "v_fmaak_f32 v152, v152, v156, 0xbfb8aa3b\n\t" \
  "v_fmaak_f32 v153, v153, v156, 0xbfb8aa3b\n\t" \
  "v_fmaak_f32 v154, v154, v156, 0xbfb8aa3b\n\t" \
  "v_fmaak_f32 v155, v155, v156, 0xbfb8aa3b\n\t" \
  "v_cvt_pk_bf16_f32 v192, v200, v201\n\t" \
  "v_cvt_pk_bf16_f32 v193, v202, v203\n\t" \
  "ds_write_b64 v224, v[192:193] offset:64\n\t" \
  "v_rcp_f32 v152, v152\n\t" \
  "v_rcp_f32 v153, v153\n\t" \
  "v_rcp_f32 v154, v154\n\t" \
  "v_rcp_f32 v155, v155\n\t" \
  "v_exp_f32 v148, v124\n\t" \
  "v_exp_f32 v149, v125\n\t" \
  "v_exp_f32 v150, v126\n\t" \
  "v_exp_f32 v151, v127\n\t" \
  "v_fmac_f32 v204, v120, v152\n\t" \
  "v_fmac_f32 v205, v121, v153\n\t" \
  "v_fmac_f32 v206, v122, v154\n\t" \
  "v_fmac_f32 v207, v123, v155\n\t" \
  "v_lshlrev_b32 v200, 16, v176\n\t" \
  "v_and_b32 v201, 0xffff0000, v176\n\t" \
  "v_lshlrev_b32 v202, 16, v177\n\t" \
  "v_and_b32 v203, 0xffff0000, v177\n\t" \
  "v_fmaak_f32 v148, v148, v156, 0xbfb8aa3b\n\t" \
  "v_fmaak_f32 v149, v149, v156, 0xbfb8aa3b\n\t" \
  "v_fmaak_f32 v150, v150, v156, 0xbfb8aa3b\n\t" \
  "v_fmaak_f32 v151, v151, v156, 0xbfb8aa3b\n\t" \
  "v_cvt_pk_bf16_f32 v194, v204, v205\n\t" \
  "v_cvt_pk_bf16_f32 v195, v206, v207\n\t" \
  "ds_write_b64 v224, v[194:195] offset:80\n\t" \
  "v_rcp_f32 v148, v148\n\t" \
  "v_rcp_f32 v149, v149\n\t" \
  "v_rcp_f32 v150, v150\n\t" \
  "v_rcp_f32 v151, v151\n\t" \
  "v_exp_f32 v152, v128\n\t" \
  "v_exp_f32 v153, v129\n\t" \
  "v_exp_f32 v154, v130\n\t" \
  "v_exp_f32 v155, v131\n\t" \
  "v_fmac_f32 v200, v124, v148\n\t" \
  "v_fmac_f32 v201, v125, v149\n\t" \
  "v_fmac_f32 v202, v126, v150\n\t" \
  "v_fmac_f32 v203, v127, v151\n\t" \
  "v_lshlrev_b32 v204, 16, v178\n\t" \
  "v_and_b32 v205, 0xffff0000, v178\n\t" \
  "v_lshlrev_b32 v206, 16, v179\n\t" \
  "v_and_b32 v207, 0xffff0000, v179\n\t" \
  "v_fmaak_f32 v152, v152, v156, 0xbfb8aa3b\n\t" \
  "v_fmaak_f32 v153, v153, v156, 0xbfb8aa3b\n\t" \
  "v_fmaak_f32 v154, v154, v156, 0xbfb8aa3b\n\t" \
  "v_fmaak_f32 v155, v155, v156, 0xbfb8aa3b\n\t" \
  "v_cvt_pk_bf16_f32 v196, v200, v201\n\t" \
  "v_cvt_pk_bf16_f32 v197, v202, v203\n\t" \
  "ds_write_b64 v224, v[196:197] offset:96\n\t" \
  "v_rcp_f32 v152, v152\n\t" \
  "v_rcp_f32 v153, v153\n\t" \
  "v_rcp_f32 v154, v154\n\t" \
  "v_rcp_f32 v155, v155\n\t" \
  "s_nop 0\n\t" \
  "v_fmac_f32 v204, v128, v152\n\t" \
  "v_fmac_f32 v205, v129, v153\n\t" \
  "v_fmac_f32 v206, v130, v154\n\t" \
  "v_fmac_f32 v207, v131, v155\n\t" \
  "v_cvt_pk_bf16_f32 v198, v204, v205\n\t" \
  "v_cvt_pk_bf16_f32 v199, v206, v207\n\t" \
  "ds_write_b64 v224, v[198:199] offset:112\n\t" \
  "9:\n\t" \
  "ds_write_b16 v229, v254 offset:5376\n\t" \
  "s_mov_b32 m0, %[keep]\n\t" \
  "s_setprio 0\n\t" \
  "s_nop 11\n\t" \
  ""

DI u32x4 tr_frag(unsigned p, int half) {
  const s16x4 lo = __builtin_amdgcn_ds_read_tr16_b64_v4i16((lds_s16x4_p)p);
  const s16x4 hi = __builtin_amdgcn_ds_read_tr16_b64_v4i16((lds_s16x4_p)(p + half));
  return __builtin_bit_cast(u32x4, __builtin_shufflevector(lo, hi, 0, 1, 2, 3, 4, 5, 6, 7));
}
DI u32x4 fold(u32x4 v) {
#pragma unroll
  for (int i = 0; i < 4; i++) v[i] = pk_bf16(bf2f(v[i] & 0xffffu) * -LOG2E, __uint_as_float(v[i] & 0xffff0000u) * -LOG2E);
  return v;
}

__global__ __launch_bounds__(512, 2) void egnn_kernel(const char* __restrict__ hmat, const char* __restrict__ pos,
                                                      const char* __restrict__ ea, const int* __restrict__ src,
                                                      const int* __restrict__ dst, const char* __restrict__ we,
                                                      const char* __restrict__ wx, const char* __restrict__ wh,
                                                      char* __restrict__ ws, uint16_t* __restrict__ out, int N, int E STAMP_ARG) {
  const int w = __builtin_amdgcn_readfirstlane(threadIdx.x >> 6);
  const int bkt = (blockIdx.x & 7) * 32 + (blockIdx.x >> 3);
  const int n0 = bkt * NB;
  STAMP(0);
  unsigned wl = L_WAVE + w * WSZ, mi = L_MI + w * MISZ;
  const auto rs_tab = __builtin_amdgcn_make_buffer_rsrc((void*)(ws + WS_TAB), 0, NBK * NBK * 8, 0x00020000);
  const auto rs_rec = __builtin_amdgcn_make_buffer_rsrc((void*)(ws + WS_REC), 0, NBK * CH * 8, 0x00020000);
  const auto rs_ea = __builtin_amdgcn_make_buffer_rsrc((void*)ea, 0, E * 32, 0x00020000);
  unsigned tag_lo, tag_hi;

  {
    const int lane = lane_id(), tid = w * 64 + lane, r = lane & 31, h = lane >> 5;
    const int e0 = bkt * CH;
    int ne = max(E - e0, 0);
    asm("" : "+s"(ne));
    ne = min(ne, CH);
    const auto rs_d = __builtin_amdgcn_make_buffer_rsrc((void*)(dst + e0), 0, ne * 4, 0x00020000);
    const auto rs_s = __builtin_amdgcn_make_buffer_rsrc((void*)(src + e0), 0, ne * 4, 0x00020000);
    unsigned d[7], sv[7], wdv0, wdv1;
    u32x2 ep, xa, xb;
#pragma unroll
    for (int i = 0; i < 7; i++)
      asm volatile("buffer_load_dword %0, %2, %3, %5 offen nt\n\tbuffer_load_dword %1, %2, %4, %5 offen nt"
                   : "=&v"(d[i]), "=&v"(sv[i]) : "v"(tid * 4), "s"(rs_d), "s"(rs_s), "s"(i * 2048) : "memory");
    LDS(unsigned, L_CNT + tid * 4) = 0;
    barrier_lds();
    asm volatile("global_load_dwordx2 %0, %5, %6 sc1\n\tglobal_load_ushort %1, %7, %8\n\tglobal_load_ushort %2, %7, %8 offset:64\n\t"
                 "global_load_dwordx2 %3, %9, %10\n\tglobal_load_dwordx2 %4, %9, %10 offset:16"
                 : "=&v"(ep), "=&v"(wdv0), "=&v"(wdv1), "=&v"(xa), "=&v"(xb)
                 : "v"(0), "s"(ws + WS_EPOCH), "v"(r * 2), "s"(we + 128 * 128), "v"(h * 8), "s"(wx + ((w + 2) & 3) * 32) : "memory");
    {
      const int j = lane >> 3, jl = (j & 3) * 128 + (lane & 7) * 16, jh = (j >> 2) * 128;
      const char* const h0 = wh + (w < 4 ? 16 * w : 64 + 32 * ((w - 4) >> 1) + 4 * (w & 1)) * 128;
      const char* const e0p = we + (w < 4 ? 64 + 16 * w : 16 * (w - 4)) * 128;
      const unsigned oh = jl + jh * (w < 4 ? 8 : 16), oe = jl + jh * (w < 4 ? 4 : 8);
      const unsigned cc0 = ((lane & 7) ^ (lane >> 4)) * 16, cc1 = ((lane & 7) ^ (4 + (lane >> 4))) * 16;
      const int nr = n0 + w + (lane & 56);
      unsigned keep;
      asm volatile("s_mov_b32 %0, m0\n\ts_mov_b32 m0, %16\n\ts_nop 0\n\t"
                   "global_load_lds_dwordx4 %1, %9\n\tglobal_load_lds_dwordx4 %1, %10 offset:1024\n\t"
                   "global_load_lds_dwordx4 %2, %11 offset:2048\n\tglobal_load_lds_dwordx4 %2, %12 offset:3072\n\t"
                   "s_add_u32 m0, %16, 0x1000\n\ts_nop 0\n\tglobal_load_lds_dwordx4 %3, %13\n\t"
                   "s_mov_b32 m0, %17\n\ts_nop 0\n\tglobal_load_lds_dwordx4 %4, %14\n\t"
                   "s_add_u32 m0, %17, 0x400\n\ts_nop 0\n\tglobal_load_lds_dwordx4 %5, %14\n\t"
                   "s_add_u32 m0, %17, 0x800\n\ts_nop 0\n\tglobal_load_lds_dwordx4 %6, %14\n\t"
                   "s_add_u32 m0, %17, 0xc00\n\ts_nop 0\n\tglobal_load_lds_dwordx4 %7, %14\n\t"
                   "s_add_u32 m0, %17, 0x1000\n\ts_nop 0\n\tglobal_load_lds_dword %8, %15\n\ts_mov_b32 m0, %0"
                   : "=&s"(keep)
                   : "v"(oh), "v"(oe), "v"(w < 2 ? (lane >> 2) * 128 + (lane & 3) * 16 : 0),
                     "v"(min(nr, N - 1) * 128 + cc0), "v"(min(nr + 64, N - 1) * 128 + cc1), "v"(min(nr + 128, N - 1) * 128 + cc0),
                     "v"(min(n0 + w + 192, N - 1) * 128 + cc1), "v"(mul24(min(n0 + w + min(lane >> 1, 24) * 8, N - 1), 6) + (lane & 1) * 2),
                     "s"(h0), "s"(h0 + (w < 4 ? 4 : 8) * 128 - 1024), "s"(e0p - 2048), "s"(e0p + (w < 4 ? 8 : 4) * 128 - 3072),
                     "s"(we + 129 * 128 + (w & 1) * 64), "s"(hmat), "s"(pos), "s"(wl), "s"(mi)
                   : "memory", "scc");
    }
    asm volatile("s_waitcnt vmcnt(15)" : "+v"(d[0]), "+v"(d[1]), "+v"(d[2]), "+v"(d[3]), "+v"(d[4]), "+v"(d[5]), "+v"(d[6]),
                 "+v"(sv[0]), "+v"(sv[1]), "+v"(sv[2]), "+v"(sv[3]), "+v"(sv[4]), "+v"(sv[5]), "+v"(sv[6]) :: "memory");
    STAMP(10);
    unsigned ent;
    auto sort = [&](auto NS_, auto FULL_) __attribute__((always_inline)) {
      constexpr int NS = decltype(NS_)::value;
      constexpr bool FULL = decltype(FULL_)::value;
      unsigned bk[NS], rank[NS];
      u32x2 rec[NS];
#pragma unroll
      for (int i = 0; i < NS; i++) {
        const int el = i * 512 + tid;
        const unsigned b = mul24(d[i], 85599u) >> 24;
        const unsigned dl = d[i] - mul24(b, NB);
        bk[i] = (FULL || el < ne ? b : NBK + lane) * 4;
        rank[i] = __hip_atomic_fetch_add(&LDS(unsigned, L_CNT + bk[i]), 8u, __ATOMIC_RELAXED, __HIP_MEMORY_SCOPE_WORKGROUP);
        rec[i][0] = e0 + el;
        rec[i][1] = ((mul24(dl >> 3, 72u) + dl + 8) << 16) | sv[i];
      }
      LDS(unsigned, L_BASE + (NBK + lane) * 4) = 0x40000000u;
      barrier_lds();
      const u32x4 cv = LDS(u32x4, L_CNT + lane * 16);
      const unsigned sum = cv[0] + cv[1] + cv[2] + cv[3];
      const unsigned ex = wave_scan_incl(sum) - sum + L_REC;
      const u32x4 bv = {ex, ex + cv[0], ex + cv[0] + cv[1], ex + cv[0] + cv[1] + cv[2]};
      LDS(u32x4, L_BASE + lane * 16) = bv;
      unsigned bs[NS];
#pragma unroll
      for (int i = 0; i < NS; i++) bs[i] = LDS(unsigned, L_BASE + bk[i]);
      ent = ((LDS(unsigned, L_BASE + (tid & 255) * 4) - L_REC) >> 3) | (LDS(unsigned, L_CNT + (tid & 255) * 4) << 9);
      const unsigned lim = L_REC + 8 * SEG + lane * 8;
#pragma unroll
      for (int i = 0; i < NS; i++) LDS(u32x2, FULL ? bs[i] + rank[i] : min(bs[i] + rank[i], lim)) = rec[i];
      barrier_lds();
    };
    if (ne == CH && w != 0) sort(std::integral_constant<int, 6>(), std::true_type());
    else sort(std::integral_constant<int, 7>(), std::false_type());
    LDS(unsigned, L_CNT + tid * 4) = 0;
    STAMP(11);
    {
      const auto rs_out = __builtin_amdgcn_make_buffer_rsrc((void*)(ws + WS_REC + (size_t)bkt * (CH * 8)), 0, ((ne + 1) >> 1) * 16, 0x00020000);
#pragma unroll
      for (int i = 0; i < 4; i++)
        __builtin_amdgcn_raw_buffer_store_b128(LDS(u32x4, L_REC + (i * 512 + tid) * 16), rs_out, (i * 512 + tid) * 16, 0, 16);
    }
    asm volatile("s_waitcnt vmcnt(4)" : "+v"(ep), "+v"(wdv0), "+v"(wdv1), "+v"(xa), "+v"(xb) :: "memory");
    {
      const unsigned tp = wl + (8 * h + ((lane >> 2) & 3)) * 128 + ((lane >> 4) & 1) * 32 + (lane & 3) * 8;
      u32x4 f[2], g[2];
#pragma unroll
      for (int t = 0; t < 2; t++) { f[t] = tr_frag(tp + 64 * t, 512); g[t] = tr_frag(tp + 2048 + 64 * t, 512); }
#pragma unroll
      for (int t = 0; t < 2; t++) {
        if (w < 4) { f[t] = fold(f[t]); g[t] = fold(g[t]); }
        LDS(u32x4, L_WH + (2 * w + t) * 1024 + lane * 16) = f[t];
        LDS(u32x4, wl + 2048 + t * 1024 + lane * 16) = g[t];
      }
      u32x4 x;
      if (w < 2) x = fold(tr_frag(wl + W_STE + (8 * h + ((lane >> 2) & 3)) * 64 + ((lane >> 4) & 1) * 32 + (lane & 3) * 8, 256));
      else { const unsigned m = ((lane & 3) == 0 && ((lane >> 2) & 1) == ((lane >> 4) & 1)) ? ~0u : 0u; x = (u32x4){xa[0] & m, xa[1] & m, xb[0] & m, xb[1] & m}; }
      if (w < 6) LDS(u32x4, L_WJ + w * 1024 + lane * 16) = x;
    }
    const unsigned long long e1 = (((unsigned long long)ep[1] << 32) | ep[0]) + 1;
    tag_lo = __builtin_amdgcn_readfirstlane((unsigned)e1 & 0xffu);
    tag_hi = __builtin_amdgcn_readfirstlane((unsigned)(e1 >> 8));
    const u32x2 g = {ent | (tag_lo << 24), tag_hi};
    asm volatile("s_waitcnt vmcnt(0) lgkmcnt(0)\n\ts_barrier" ::: "memory");
    if (tid < NBK) __builtin_amdgcn_raw_buffer_store_b64(g, rs_tab, tid * (NBK * 8) + bkt * 8, 0, 16);
    STAMP(12);
    {
      u32x2 hb[8];
#pragma unroll
      for (int i = 0; i < 8; i++) hb[i] = LDS(u32x2, mi + r * 128 + ((i ^ ((r >> 1) & 7)) * 16) + h * 8);
#pragma unroll
      for (int t = 0; t < 2; t++) {
        f32x16 P;
#pragma unroll
        for (int i = 0; i < 16; i++) P[i] = 0.f;
#pragma unroll
        for (int s = 0; s < 4; s++) {
          const u32x4 a = {hb[2 * s][0], hb[2 * s][1], hb[2 * s + 1][0], hb[2 * s + 1][1]};
          P = MFMA(as_bf(a), LDS(bf16x8, L_WAVE + (4 + s) * WSZ + 2048 + t * 1024 + lane * 16), P);
        }
        P[15] = bf2f(t ? wdv1 : wdv0);
        P[14] = 0.5f * P[15];
#pragma unroll
        for (int i = 0; i < 16; i++) P[i] *= -LOG2E;
#pragma unroll
        for (int s = 0; s < 2; s++) {
          const u32x4 a = {pk_bf16(P[8 * s], P[8 * s + 1]), pk_bf16(P[8 * s + 2], P[8 * s + 3]),
                           pk_bf16(P[8 * s + 4], P[8 * s + 5]), pk_bf16(P[8 * s + 6], P[8 * s + 7])};
          LDS(u32x4, mi + (2 * s + t) * 1024 + lane * 16) = a;
        }
      }
    }
    if (tid >= 256 && tid < 256 + 26 * 4) {
      const int t = tid - 256, row = t >> 2, sx = (t >> 1) & 1, hh = t & 1;
      const int x = row < DUMMY ? row - 16 * sx - 4 * hh : -1;
      u32x4 v;
      v[0] = x == 0 ? 0x3f80u : x == 1 ? 0x3f800000u : 0u;  v[1] = x == 2 ? 0x3f80u : x == 3 ? 0x3f800000u : 0u;
      v[2] = x == 8 ? 0x3f80u : x == 9 ? 0x3f800000u : 0u;  v[3] = x == 10 ? 0x3f80u : x == 11 ? 0x3f800000u : 0u;
      LDS(u32x4, L_LUT + row * 80 + (t & 3) * 16) = v;
    }
    {
      const u32x4 z4 = {0u, 0u, 0u, 0u};
#pragma unroll
      for (int i = 0; i < 3; i++) { const int o = i * 1024 + lane * 16; if (o < 26 * 80 + 320) LDS(u32x4, wl + W_SI + o) = z4; }
      const u32x2 pg = LDS(u32x2, mi + 4096 + r * 8);
      const float pz = __uint_as_float(pg[1] & 0xffff0000u);
      const f32x4 pv = {bf2f(pg[0] & 0xffffu), pz, __uint_as_float(pg[0] & 0xffff0000u), pz};
      if (lane < DUMMY) LDS(f32x4, wl + W_SI + lane * 80 + 64) = pv;
    }
    STAMP(14);
  }

  f32x16 Y0, Y1;
  f32x4 Yp = {0.f, 0.f, 0.f, 0.f};
#pragma unroll
  for (int i = 0; i < 16; i++) { Y0[i] = 0.f; Y1[i] = 0.f; }
  u32x32 W2; u32x16 PA;
  int jbase = 0, part = -1, grp = w, gother = w;

  auto round = [&](bool first) __attribute__((always_inline)) -> bool {
    const int lane = lane_id(), tid = w * 64 + lane, r = lane & 31, h = lane >> 5;
    const unsigned zr = opaque(0);
    if (!first) { LDS(unsigned, L_CNT + tid * 4) = zr; barrier_lds(); }
    LDS(unsigned, L_BASE + lane * 4) = 0x40000000u;
    unsigned tb[8];
    {
      u32x2 t;
      for (int spin = 0;; spin++) {
        asm volatile("buffer_load_dwordx2 %0, %1, %2, %3 offen sc1\n\ts_waitcnt vmcnt(0)"
                     : "=&v"(t) : "v"(w * 256 + r * 8), "s"(rs_tab), "s"(bkt * (NBK * 8)) : "memory");
        if (__builtin_amdgcn_ballot_w64(t[1] != tag_hi || (t[0] >> 24) != tag_lo) == 0) break;
        if (spin > (1 << 20)) { t[0] = 0; break; }
        __builtin_amdgcn_s_sleep(POLL_SLEEP);
      }
      LDS(unsigned, L_WAVE + w * WSZ + W_STE + r * 4) = t[0];
      const u32x4 t0 = LDS(u32x4, L_WAVE + w * WSZ + W_STE + (lane >> 4) * 32), t1 = LDS(u32x4, L_WAVE + w * WSZ + W_STE + (lane >> 4) * 32 + 16);
#pragma unroll
      for (int p = 0; p < 4; p++) { tb[p] = t0[p]; tb[4 + p] = t1[p]; }
    }
    STAMP(8);
    u32x2 rc[10];
    unsigned lmax = 0, nov = 0;
    bool big;
    {
      const unsigned jj = lane & (LG - 1), j = jbase + jj, o0 = mul24((unsigned)tid / LG, 8 * CH * 8) + j * 8, oob = 0x80000000u + zr;
      unsigned off[8], len[8];
#pragma unroll
      for (int p = 0; p < 8; p++) {
        len[p] = (tb[p] >> 12) & 0xfffu;
        if (part >= 0 && part != 2 * p + (w & 1)) len[p] = 0;
        off[p] = o0 + (tb[p] & 0xfffu) * 8;
        const unsigned a = j < len[p] ? off[p] : oob;
        asm volatile("buffer_load_dwordx2 %0, %1, %2, %3 offen sc1" : "=v"(rc[p]) : "v"(a), "s"(rs_rec), "s"(p * CH * 8) : "memory");
      }
      asm volatile("" : "+v"(len[0]), "+v"(len[1]), "+v"(len[2]), "+v"(len[3]), "+v"(len[4]), "+v"(len[5]), "+v"(len[6]), "+v"(len[7]));
      unsigned a9 = oob;
#pragma unroll
      for (int p = 0; p < 8; p++) {
        const unsigned ov = (unsigned)min(max((int)len[p] - (jbase + LG), 0), LG);
        a9 = jj - nov < ov ? off[p] - nov * 8 + (p * CH * 8 + LG * 8) : a9;
        nov += ov;
      }
      asm volatile("buffer_load_dwordx2 %0, %1, %2, 0 offen sc1" : "=v"(rc[8]) : "v"(a9), "s"(rs_rec) : "memory");
#pragma unroll
      for (int p = 0; p < 8; p++) lmax = max(lmax, tb[p] & 0xfff000u);
      rc[9] = (u32x2){zr, zr};
      big = __builtin_amdgcn_ballot_w64(nov > LG) != 0;
      if (big) {
        unsigned a10 = oob, s1 = 0;
#pragma unroll
        for (int p = 0; p < 8; p++) {
          const unsigned ov = (unsigned)min(max((int)len[p] - (jbase + LG), 0), LG);
          a10 = jj + LG - s1 < ov ? off[p] - s1 * 8 + (p * CH * 8 + LG * 16) : a10;
          s1 += ov;
        }
        asm volatile("buffer_load_dwordx2 %0, %1, %2, 0 offen sc1" : "+v"(rc[9]) : "v"(a10), "s"(rs_rec) : "memory");
      }
    }
    unsigned sink = zr;
    STAMP(7);
    if (first) {
#pragma unroll
      for (int i = 0; i < 8; i++) {
        const u32x4 v = LDS(u32x4, L_WAVE + (i >> 1) * WSZ + 2048 + (i & 1) * 1024 + lane * 16);
#pragma unroll
        for (int j = 0; j < 4; j++) W2[4 * i + j] = v[j];
      }
    }
    STAMP(13);
    if ((lmax >> 12) > (unsigned)jbase + 2 * LG || nov > 2 * LG) __hip_atomic_fetch_or(&LDS(unsigned, L_FLAG), nov > 2 * LG ? 3u : 1u, __ATOMIC_RELAXED, __HIP_MEMORY_SCOPE_WORKGROUP);
    unsigned mk1 = (lane & 7) >= 1 ? ~zr : zr, mk2 = (lane & 7) >= 2 ? ~zr : zr, sg0 = L_REC + (lane >> 3) * SEG;
    unsigned lim = L_REC + 8 * SEG + lane * 8, own = n0 + min(8 * (r & 3) + (r >> 2), 24) * 8;
    unsigned ce = ((lane & 1) ^ ((lane >> 4) & 1)) * 16, cp = (lane & 1) * 2;
    unsigned cc0 = ((lane & 7) ^ (lane >> 4)) * 16, cc1 = ((lane & 7) ^ (4 + (lane >> 4))) * 16;
    unsigned q_s4 = L_REC + (lane >> 3) * 32 + 4;
    unsigned q_r2 = L_REC + (4 * ((lane >> 1) & 7) + (lane >> 4)) * 8;
    unsigned q_r1 = L_REC + (4 * (r & 7) + (r >> 3)) * 8 + 4;
    asm volatile("" : "+v"(mk1), "+v"(mk2), "+v"(sg0), "+v"(lim), "+v"(own), "+v"(ce), "+v"(cp), "+v"(cc0), "+v"(cc1), "+v"(q_s4),
                 "+v"(q_r2), "+v"(q_r1));
    if (big) asm volatile("s_waitcnt vmcnt(2)" ::: "memory"); else asm volatile("s_waitcnt vmcnt(1)" ::: "memory");
    asm volatile("" : "+v"(rc[0]), "+v"(rc[1]), "+v"(rc[2]), "+v"(rc[3]), "+v"(rc[4]), "+v"(rc[5]), "+v"(rc[6]), "+v"(rc[7]) :: "memory");
    STAMP(1);
    unsigned rk[10], ky[10];
#pragma unroll
    for (int p = 0; p < 10; p++) {
      if (p == 8) asm volatile("s_waitcnt vmcnt(0)" : "+v"(rc[8]), "+v"(rc[9]) :: "memory");
      if (p == 9 && !big) { ky[p] = 0; rk[p] = 0; break; }
      ky[p] = max((rc[p][1] >> 9) & 0x7fcu, (unsigned)lane * 4);
      rk[p] = __hip_atomic_fetch_add(&LDS(unsigned, L_CNT + ky[p]), 8u + zr, __ATOMIC_RELAXED, __HIP_MEMORY_SCOPE_WORKGROUP);
    }
    if (EPF) {
#pragma unroll
      for (int p = 0; p < 9; p++) {
        const unsigned a = (ky[p] & 0x47cu) - 0x400u < (unsigned)EPF * 4 ? rc[p][0] << 5 : 0x80000000u + zr;
        asm volatile("buffer_load_dword %0, %1, %2, 0 offen" : "+{v3}"(sink) : "v"(a), "s"(rs_ea) : "memory");
      }
    }
    barrier_lds();
    STAMP(2);
    unsigned g;
    const unsigned flagv = LDS(unsigned, L_FLAG);
    {
      const u32x4 cv = LDS(u32x4, L_CNT + 1024 + lane * 16);
      const unsigned sum = cv[0] + cv[1] + cv[2] + cv[3];
      g = sum + (__builtin_amdgcn_update_dpp(0, (int)sum, 0x111, 0xf, 0xf, true) & mk1);
      g += __builtin_amdgcn_update_dpp(0, (int)g, 0x112, 0xf, 0xf, true) & mk2;
      g += __builtin_amdgcn_update_dpp(0, (int)g, 0x114, 0xf, 0xa, false);
      const unsigned ex = g - sum + sg0;
      const u32x4 bv = {ex, ex + cv[0], ex + cv[0] + cv[1], ex + cv[0] + cv[1] + cv[2]};
      LDS(u32x4, L_BASE + 1024 + lane * 16) = bv;
    }
    unsigned bs[9];
#pragma unroll
    for (int p = 0; p < 9; p++) bs[p] = LDS(unsigned, L_BASE + ky[p]);
    const unsigned flag = __builtin_amdgcn_readfirstlane(flagv);
    const bool again = flag & 1;
    const bool over = (flag & 2) || __builtin_amdgcn_ballot_w64(g > (unsigned)(SEG - 256)) != 0;
    if (!over) {
#pragma unroll
      for (int p = 0; p < 9; p++) LDS(u32x2, min(bs[p] + rk[p], lim)) = rc[p];
      if (big) LDS(u32x2, min(LDS(unsigned, L_BASE + ky[9]) + rk[9], lim)) = rc[9];
    }
    if (first) {
      const unsigned key = (g << 3) | (lane >> 3);
      unsigned ks[8], rank = 0;
#pragma unroll
      for (int k = 0; k < 8; k++) ks[k] = __builtin_amdgcn_readlane(key, 8 * k + 7);
#pragma unroll
      for (int k = 0; k < 8; k++) rank += ks[k] > key;
      const unsigned long long sel = 0x8080808080808080ull;
      grp = __builtin_ctzll(__builtin_amdgcn_ballot_w64(rank == (unsigned)(w < 4 ? w : 11 - w)) & sel) >> 3;
      gother = __builtin_ctzll(__builtin_amdgcn_ballot_w64(rank == (unsigned)(w < 4 ? 7 - w : w - 4)) & sel) >> 3;
      wl = L_WAVE + grp * WSZ; mi = L_MI + grp * MISZ;
    }
    unsigned segn = over ? 0u : (unsigned)__builtin_amdgcn_readlane(g, 8 * grp + 7);
    const unsigned seg0 = grp * SEG;
    const int nt = (segn + 255) >> 8;
    {
      const unsigned dum = (DUMMY * 80u + 8 + grp) << 16;
      const u32x2 pad = {zr, dum + zr}, row = {zr, min(own + grp, (unsigned)N - 1) | dum};
      LDS(u32x2, L_REC + seg0 + segn + r * 8) = pad;
      LDS(u32x2, L_REC + seg0 + nt * 256 + r * 8) = row;
    }
    barrier_lds();
    STAMP(3);

    const unsigned o_s4 = q_s4 + seg0, o_r2 = q_r2 + seg0, o_r1 = q_r1 + seg0;
    {
      unsigned ns[4];
#pragma unroll
      for (int i = 0; i < 4; i++) ns[i] = LDS(uint16_t, o_s4 + i * 8);
      const u32x2 nr2 = LDS(u32x2, o_r2);
      dma_tile(hmat, ea, pos, ns[0] * 128 + cc0, ns[1] * 128 + cc1, ns[2] * 128 + cc0, ns[3] * 128 + cc1,
               (nr2[0] << 5) | ce, (nr2[1] & 0xffffu) * 6 + cp, wl);
    }
    if (first) {
#pragma unroll
      for (int i = 0; i < 4; i++) {
        const u32x4 v = LDS(u32x4, mi + i * 1024 + lane * 16);
#pragma unroll
        for (int j = 0; j < 4; j++) PA[4 * i + j] = v[j];
      }
      if (bkt == 0 && tid == 0) {
        const u32x2 e1 = {tag_lo | (tag_hi << 8), tag_hi >> 24};
        asm volatile("global_store_dwordx2 %0, %1, %2 sc1" :: "v"(zr), "v"(e1), "s"(ws + WS_EPOCH) : "memory");
      }
    }
    const unsigned nother = (__builtin_amdgcn_readlane(g, 8 * gother + 7) + 255) >> 8;
    const bool last_round = !over && !again && (part < 0 || part == 15);
    STAMP(4);

    {
      u32x16 A0, B0; u32x8 A1;
      const unsigned bias = 8 + grp;
#pragma unroll
      for (int s = 0; s < 4; s++) A0[s] = wl + W_STH + r * 128 + (((2 * s + h) ^ ((r >> 1) & 7)) * 16);
      A0[4] = wl + r * 32 + ((h ^ ((r >> 3) & 1)) * 16);
      A0[5] = wl + r * 8;
      A0[6] = wl + r * 8 + h * 4;
      A0[7] = L_LUT + h * 16 - bias;
      A0[8] = wl + r * 2 - bias;
      A0[9] = wl + min(r, DUMMY) * 80 + h * 16;
      A0[10] = mi + r * MIL + h * 32;
      A0[11] = mi + (8 * h + ((lane >> 2) & 3)) * MIL + (4 * ((lane >> 4) & 1) + (lane & 3)) * 8;
      A0[12] = wl + h * 128 + r * 2;
      {
        const int c = lane & 3, ok = c < 3 && (lane & 8) == 0 && ((lane >> 2) & 1) == ((lane >> 4) & 1);
        A0[13] = wl + (ok ? (c == 0 ? 0 : c == 1 ? 128 : 64) : 256) + h * 16;
      }
      A0[14] = L_WJ + lane * 16;
      A0[15] = wl + W_SI + 64 + h * 8 - bias;
#pragma unroll
      for (int i = 0; i < 16; i++) B0[i] = zr;
      B0[1] = cc0 + 3072; B0[3] = cc1 + 2048; B0[5] = cc0 + 1024; B0[7] = cc1; B0[9] = ce; B0[11] = cp;
      A1[0] = o_s4 + 256; A1[1] = o_r2 + 256; A1[2] = o_r1 + 2; A1[3] = zr;
      A1[4] = (h ? 0u : 0x3f80u) + zr; A1[5] = zr; A1[6] = 0x3f800000u + zr; A1[7] = zr;
      const auto rsh = __builtin_amdgcn_make_buffer_rsrc((void*)(hmat - 3072), 128, N, 0x00020000);
      const auto rse = __builtin_amdgcn_make_buffer_rsrc((void*)ea, 32, E, 0x00020000);
      const auto rsp = __builtin_amdgcn_make_buffer_rsrc((void*)(pos - 1024), 6, N, 0x00020000);
      unsigned cnt = nt, keep;
#define PRIO_A 14
#define PRIO_B 7
      const int lastv = __builtin_amdgcn_readfirstlane(opaque(last_round ? 1 : 0));
      const int thr = __builtin_amdgcn_readfirstlane(w >= 4 ? max((int)(nother * PRIO_A - cnt * PRIO_B) >> 5, 0) : -1);
      asm volatile(LOOP_ASM
                   : "+{v[52:67]}"(Y0), "+{v[68:83]}"(Y1), "+{v[84:87]}"(Yp), "+{v[216:231]}"(B0), "+{v[232:247]}"(A0), "+{v[248:255]}"(A1),
                     "+{v3}"(sink), [nt] "+s"(cnt), [keep] "=&s"(keep)
                   : "{v[4:35]}"(W2), "{v[36:51]}"(PA), [rsh] "s"(rsh), [rse] "s"(rse), [rsp] "s"(rsp),
                     [b1] "s"(wl), [thr] "s"(thr), [last] "s"(lastv)
                   : "memory", "scc", "vcc", CLOBBER_V);
    }
    if (over) part = 0;
    else if (part >= 0 && part < 15) part++;
    else { part = -1; jbase += 2 * LG; }
    return last_round;
  };
  if (!round(true)) {
    do {
      wait_vm0();
      barrier_lds();
      jbase = __builtin_amdgcn_readfirstlane(jbase); part = __builtin_amdgcn_readfirstlane(part);
    } while (!round(false));
  }
  STAMP(5);

  {
    const int lane = lane_id(), r = lane & 31, h = lane >> 5;
    Yp[0] *= -LN2; Yp[1] *= -LN2; Yp[2] *= -LN2;
    const auto rs_o = __builtin_amdgcn_make_buffer_rsrc((void*)out, 0, N * 128, 0x00020000);
#pragma unroll
    for (int i = 0; i < 4; i++) {
      const int q = 8 * i + (lane >> 3), dl = q * 8 + grp;
      const u32x4 v = LDS(u32x4, mi + q * MIP + (lane & 7) * 16);
      if (dl < NB) __builtin_amdgcn_raw_buffer_store_b128(v, rs_o, (n0 + dl) * 128 + (lane & 7) * 16, 0, OUT_AUX);
    }
    const int dl = r * 8 + grp;
    if (h == 0 && dl < NB && n0 + dl < N) {
      const f32x4 pd = LDS(f32x4, wl + W_SI + r * 80 + 64);
      uint16_t* po = out + (size_t)N * 64 + (size_t)(n0 + dl) * 3;
      const unsigned xy = pk_bf16(pd[0] + Yp[0], pd[2] + Yp[1]), zz = pk_bf16(pd[1] + Yp[2], 0.f);
      po[0] = (uint16_t)xy; po[1] = (uint16_t)(xy >> 16); po[2] = (uint16_t)zz;
    }
  }
  STAMP(6);
}

}

#define STAMP_PASS1
#define STAMP_PASS2

extern "C" void kernel_launch(void* const* d_in, const int* in_sizes, int n_in, void* d_out, int out_size,
                              void* d_ws, size_t ws_size, hipStream_t stream) {
  const int N = in_sizes[0] / 64, E = in_sizes[3];
  if (N > NBK * NB || E > NBK * CH || N < 1 || E < 1) return;
  (void)hipFuncSetAttribute((const void*)egnn_kernel, hipFuncAttributeMaxDynamicSharedMemorySize, L_TOTAL);
  egnn_kernel<<<NBK, 512, L_TOTAL, stream>>>((const char*)d_in[0], (const char*)d_in[1], (const char*)d_in[2], (const int*)d_in[3],
                                             (const int*)d_in[4], (const char*)d_in[5], (const char*)d_in[6], (const char*)d_in[7],
                                             (char*)d_ws, (uint16_t*)d_out, N, E STAMP_PASS2);
}
```
